```python
import torch

def _softmax(x, axis=-1):
    return torch.softmax(x, dim=axis)
def _gelu(x):
    return torch.nn.functional.gelu(x, approximate="tanh")
def _silu(x):
    return torch.nn.functional.silu(x)
def _sigmoid(x):
    return torch.sigmoid(x)
def _layernorm(x, axis=-1, eps=1e-5):
    m = x.mean(dim=axis, keepdim=True)
    v = x.var(dim=axis, keepdim=True, unbiased=False)
    return (x - m) / torch.sqrt(v + eps)
def _scatter_add(tgt, idx, src, axis=0):
    return tgt.clone().index_add_(axis, idx, src)
def _segment_sum(src, seg, n):
    out = torch.zeros((n,) + src.shape[1:], dtype=src.dtype)
    return out.index_add_(0, seg, src)
def _pad(x, pw):
    flat = tuple(v for pair in reversed(pw) for v in pair)
    return torch.nn.functional.pad(x, flat)

kernel_name = 'recurrent_scan__sliding_local_dilated__dense_activation_gelu__prompt'

def setup_inputs():

    _w = (lambda a: (lambda t: t.to(torch.bfloat16) if t.is_floating_point() else t)(torch.as_tensor(a)))
    def _A_setup():
        B, S, D = 1, 2048, 640
        return {
            "x": _w((lambda *s: torch.randn(*s, dtype=torch.bfloat16) * 0.05)(B, S, D)),
            "wqkv": _w((lambda *s: torch.randn(*s, dtype=torch.bfloat16) * 0.2)(D, 3 * D)),
            "wo": _w((lambda *s: torch.randn(*s, dtype=torch.bfloat16) * 0.2)(D, D)),
        }
    _A_in = _A_setup()

    def _B_setup():
        B, S, D, F = 1, 2048, 640, 1728
        return {
            "x": _w((lambda *s: torch.randn(*s, dtype=torch.bfloat16) * 0.05)(B, S, D)),
            "w1": _w((lambda *s: torch.randn(*s, dtype=torch.bfloat16) * 1.6)(D, F) * (D ** -0.5)),
            "b1": _w((lambda *s: torch.randn(*s, dtype=torch.bfloat16) * 1.6)(F) * 0.01),
            "w2": _w((lambda *s: torch.randn(*s, dtype=torch.bfloat16) * 0.1)(F, D) * (F ** -0.5)),
            "b2": _w((lambda *s: torch.randn(*s, dtype=torch.bfloat16) * 0.1)(D) * 0.01),
        }
    _B_in = _B_setup()

    return {
        "A_x": _A_in["x"],
        "A_wqkv": _A_in["wqkv"],
        "A_wo": _A_in["wo"],
        "B_x": _B_in["x"],
        "B_w1": _B_in["w1"],
        "B_b1": _B_in["b1"],
        "B_w2": _B_in["w2"],
        "B_b2": _B_in["b2"],
    }

def reference(A_x, A_wqkv, A_wo, B_x, B_w1, B_b1, B_w2, B_b2):
    def _A_ref(x, wqkv, wo):
        B, S, D = x.shape
        H = 32; Dh = D // H
        W, dil = 8, 2
        qkv = (lambda x, *d: x.permute(*d))((x @ wqkv).reshape(B, S, 3, H, Dh), 2, 0, 3, 1, 4)
        q, k, v = qkv[0], qkv[1], qkv[2]
        scale = Dh ** -0.5
        pos = torch.arange(S)
        off = torch.arange(W) * dil
        idx = pos[:, None] - off[None, :]
        mask = idx >= 0
        idx_c = torch.where(mask, idx, 0).reshape(-1)
        kg = k[:, :, idx_c, :].reshape(B, H, S, W, Dh)
        vg = v[:, :, idx_c, :].reshape(B, H, S, W, Dh)
        logits = torch.einsum("bhsd,bhswd->bhsw", q, kg) * scale
        logits = torch.where(mask[None, None, :, :], logits, -1e9)
        attn = _softmax(logits, axis=-1)
        o = torch.einsum("bhsw,bhswd->bhsd", attn, vg)
        return (lambda x, *d: x.permute(*d))(o, 0, 2, 1, 3).reshape(B, S, D) @ wo

    def _B_ref(x, w1, b1, w2, b2):
        h = torch.einsum("bsd,df->bsf", x, w1) + b1
        h = _gelu(h)
        return torch.einsum("bsf,fd->bsd", h, w2) + b2

    x = A_x
    xn = _layernorm(x, axis=-1)
    s = _A_ref(xn, A_wqkv, A_wo)
    g = _silu(_B_ref(xn, B_w1, B_b1, B_w2, B_b2))
    return x + s * g
```

```cpp
#include <hip/hip_runtime.h>
#include <hip/hip_bf16.h>
typedef unsigned short u16;
__device__ __forceinline__ float bf2f(u16 v){ return __uint_as_float(((unsigned)v)<<16); }
__device__ __forceinline__ u16 f2bf(float f){ __hip_bfloat16 b = __float2bfloat16(f); return *(u16*)&b; }
constexpr int S=2048,D=640,F=1728,H=32,DH=20;

__global__ void k_ln(const u16* x, u16* xn){
  int r=blockIdx.x; int t=threadIdx.x;
  float s=0,s2=0; float v[10];
  for(int i=0;i<10;i++){ v[i]=bf2f(x[r*D+t+64*i]); s+=v[i]; }
  for(int o=32;o;o>>=1) s+=__shfl_xor(s,o);
  float m=s/D;
  for(int i=0;i<10;i++){ float d=v[i]-m; s2+=d*d; }
  for(int o=32;o;o>>=1) s2+=__shfl_xor(s2,o);
  float rs=rsqrtf(s2/D+1e-5f);
  for(int i=0;i<10;i++) xn[r*D+t+64*i]=f2bf((v[i]-m)*rs);
}
__global__ void k_gemm(const u16* A,const u16* B,const u16* bias,void* C,int N,int K,int mode){
  int n=blockIdx.x*64+threadIdx.x%64; int m=blockIdx.y*4+threadIdx.x/64;
  if(n>=N) return;
  float acc=0;
  for(int k=0;k<K;k++) acc+=bf2f(A[m*K+k])*bf2f(B[k*N+n]);
  if(mode==0) ((u16*)C)[m*N+n]=f2bf(acc);
  else if(mode==1){ float h=acc+bf2f(bias[n]); float u=0.7978845608f*(h+0.044715f*h*h*h); ((u16*)C)[m*N+n]=f2bf(0.5f*h*(1.f+tanhf(u))); }
  else ((float*)C)[m*N+n]=acc;
}
__global__ void k_attn(const u16* qkv,u16* o){
  int idx=blockIdx.x*blockDim.x+threadIdx.x; int s=idx/H,h=idx%H;
  float lg[8]; float mx=-1e30f;
  for(int w=0;w<8;w++){ int p=s-2*w; float a=-1e9f; if(p>=0){ a=0; for(int d=0;d<DH;d++) a+=bf2f(qkv[s*1920+h*DH+d])*bf2f(qkv[p*1920+640+h*DH+d]); a*=0.22360679775f;} lg[w]=a; mx=fmaxf(mx,a);}
  float sum=0; for(int w=0;w<8;w++){ lg[w]=__expf(lg[w]-mx); sum+=lg[w]; }
  for(int d=0;d<DH;d++){ float a=0; for(int w=0;w<8;w++){ int p=s-2*w; if(p<0)p=0; a+=lg[w]*bf2f(qkv[p*1920+1280+h*DH+d]); } o[s*D+h*DH+d]=f2bf(a/sum); }
}
__global__ void k_fin(const u16* x,const float* s,const float* y,const u16* b2,u16* out){
  int i=blockIdx.x*blockDim.x+threadIdx.x; int n=i%D;
  float yy=y[i]+bf2f(b2[n]); float g=yy/(1.f+__expf(-yy));
  out[i]=f2bf(bf2f(x[i])+s[i]*g);
}
extern "C" void kernel_launch(void* const* d_in, const int* in_sizes, int n_in, void* d_out, int out_size, void* d_ws, size_t ws_size, hipStream_t stream) {
  const u16* x=(const u16*)d_in[0]; const u16* wqkv=(const u16*)d_in[1]; const u16* wo=(const u16*)d_in[2];
  const u16* w1=(const u16*)d_in[4]; const u16* b1=(const u16*)d_in[5]; const u16* w2=(const u16*)d_in[6]; const u16* b2=(const u16*)d_in[7];
  char* ws=(char*)d_ws;
  u16* xn=(u16*)ws; u16* qkv=(u16*)(ws+(4<<20)); u16* h=(u16*)(ws+(16<<20)); u16* o=(u16*)(ws+(32<<20)); float* s=(float*)(ws+(40<<20)); float* y=(float*)(ws+(48<<20));
  k_ln<<<S,64,0,stream>>>(x,xn);
  k_gemm<<<dim3(30,S/4),256,0,stream>>>(xn,wqkv,nullptr,qkv,1920,640,0);
  k_gemm<<<dim3(27,S/4),256,0,stream>>>(xn,w1,b1,h,F,640,1);
  k_attn<<<S*H/256,256,0,stream>>>(qkv,o);
  k_gemm<<<dim3(10,S/4),256,0,stream>>>(o,wo,nullptr,s,640,640,2);
  k_gemm<<<dim3(10,S/4),256,0,stream>>>(h,w2,nullptr,y,640,F,2);
  k_fin<<<S*D/256,256,0,stream>>>(x,s,y,b2,(u16*)d_out);
}
```

```cpp
#include <hip/hip_runtime.h>
#include <hip/hip_bf16.h>
#pragma clang diagnostic ignored "-Wint-to-pointer-cast"
typedef unsigned short u16;
typedef short s16x4 __attribute__((ext_vector_type(4)));
typedef __bf16 bf16x2 __attribute__((ext_vector_type(2)));
typedef __bf16 bf16x8 __attribute__((ext_vector_type(8)));
typedef float f32x4 __attribute__((ext_vector_type(4)));
typedef float f32x2 __attribute__((ext_vector_type(2)));
typedef unsigned u32x2 __attribute__((ext_vector_type(2)));
typedef unsigned u32x4 __attribute__((ext_vector_type(4)));
#define LDS __attribute__((address_space(3)))
#define DEV __device__ __forceinline__

constexpr int D_ = 640, F_ = 1728, NQKV = 1920;
constexpr int STG = 49152;
constexpr int OFF_ROW = 3 * STG;
constexpr int OFF_CS = OFF_ROW + 1024;
constexpr int OFF_BIAS = OFF_CS + 1024;
constexpr int LDS1 = OFF_BIAS + 1024;
constexpr int NKS = 74;

DEV void dma16(unsigned lds, unsigned voff, const void* base) {
  asm volatile("s_mov_b32 m0, %0\n\ts_nop 0\n\tglobal_load_lds_dwordx4 %1, %2" ::"s"(lds), "v"(voff), "s"(base) : "memory");
}
DEV f32x4 mfma(u32x4 a, u32x4 b, f32x4 c) {
  return __builtin_amdgcn_mfma_f32_16x16x32_bf16(__builtin_bit_cast(bf16x8, a), __builtin_bit_cast(bf16x8, b), c, 0, 0, 0);
}
DEV u32x2 trread(unsigned addr) {
  return __builtin_bit_cast(u32x2, __builtin_amdgcn_ds_read_tr16_b64_v4i16((LDS s16x4*)addr));
}
DEV u32x4 ldsread128(unsigned addr) { return *(LDS u32x4*)addr; }
DEV f32x4 ldsreadf4(unsigned addr) { return *(LDS f32x4*)addr; }
DEV unsigned pk(float a, float b) { f32x2 v = {a, b}; return __builtin_bit_cast(unsigned, __builtin_convertvector(v, bf16x2)); }
DEV float dot2(unsigned a, unsigned b, float c) {
  return __builtin_amdgcn_fdot2_f32_bf16(__builtin_bit_cast(bf16x2, a), __builtin_bit_cast(bf16x2, b), c, false);
}
DEV float bf2f(u16 v) { return __uint_as_float(((unsigned)v) << 16); }
#define SB() __builtin_amdgcn_sched_barrier(0)
#define BAR() asm volatile("s_waitcnt lgkmcnt(0)\n\ts_barrier" ::: "memory")
DEV float xmax(float v) {
  unsigned u = __float_as_uint(v);
  auto r = __builtin_amdgcn_permlane32_swap(u, u, false, false);
  v = fmaxf(__uint_as_float(r[0]), __uint_as_float(r[1]));
  u = __float_as_uint(v);
  auto r2 = __builtin_amdgcn_permlane16_swap(u, u, false, false);
  return fmaxf(__uint_as_float(r2[0]), __uint_as_float(r2[1]));
}
DEV float xsum(float v) {
  unsigned u = __float_as_uint(v);
  auto r = __builtin_amdgcn_permlane32_swap(u, u, false, false);
  v = __uint_as_float(r[0]) + __uint_as_float(r[1]);
  u = __float_as_uint(v);
  auto r2 = __builtin_amdgcn_permlane16_swap(u, u, false, false);
  return __uint_as_float(r2[0]) + __uint_as_float(r2[1]);
}

template <bool QKV>
DEV void mainloop(f32x4 (&acc)[8][4], const unsigned (&wb)[8], unsigned xb0, unsigned xb1) {
  u32x4 wf[4], xf[2][4];
  auto rdW = [&](int ts, unsigned so) {
    unsigned a = wb[ts & 7] + so + 16384 + (ts >> 3) * 16384;
    u32x2 lo = trread(a), hi = trread(a + 2048);
    wf[ts & 3] = u32x4{lo[0], lo[1], hi[0], hi[1]};
  };
  BAR();
  SB();
#pragma unroll
  for (int tb = 0; tb < 4; tb++) xf[0][tb] = ldsread128(xb0 + tb * 2048);
  rdW(0, 0); rdW(1, 0); rdW(2, 0);
  unsigned so = 0;
#pragma unroll 1
  for (int s = 0; s < 10; s++) {
    unsigned sn = so + STG; if (sn == 3 * STG) sn = 0;
#pragma unroll
    for (int ts = 0; ts < 16; ts++) {
      SB();
      if (ts == 14) {
        BAR();
        SB();
#pragma unroll
        for (int tb = 0; tb < 4; tb++) xf[0][tb] = ldsread128(xb0 + sn + tb * 2048);
        rdW(0, sn); rdW(1, sn);
      } else if (ts == 15) {
        rdW(2, sn);
      } else if (ts <= 12) {
        rdW(ts + 3, so);
      }
      if (ts >= 2 && ts < 6) xf[1][ts - 2] = ldsread128(xb1 + so + (ts - 2) * 2048);
      SB();
      const int nt = ts & 7;
#pragma unroll
      for (int tb = 0; tb < 4; tb++) {
        if (QKV && nt >= 5) acc[nt][tb] = mfma(xf[ts >> 3][tb], wf[ts & 3], acc[nt][tb]);
        else acc[nt][tb] = mfma(wf[ts & 3], xf[ts >> 3][tb], acc[nt][tb]);
      }
    }
    so = sn;
  }
  SB();
}

__global__ __launch_bounds__(512, 2) void k1(const u16* __restrict__ x, const u16* __restrict__ wqkv, const u16* __restrict__ w1,
                                             const u16* __restrict__ b1, const u16* __restrict__ wo, const u16* __restrict__ w2,
                                             char* __restrict__ Aws, char* __restrict__ W2t) {
  extern __shared__ __attribute__((aligned(16))) char smem[];
  const unsigned L0 = (unsigned)(size_t)(LDS char*)smem;
  const int tid = threadIdx.x, lane = tid & 63;
  const int wid = __builtin_amdgcn_readfirstlane(tid >> 6);
  const int b = blockIdx.x, xcd = b & 7, j = b >> 3, p = xcd & 1, qd = xcd >> 1;
  const bool isq = j < 18;
  int rt, i0, colb;
  int hg = 0;
  if (isq) { hg = 2 * qd + (j >= 9); rt = j >= 9 ? j - 9 : j; i0 = 112 * rt; colb = 80 * hg; }
  else { int u = 14 * qd + (j - 18); int ct = u >> 3; rt = u & 7; i0 = 128 * rt; colb = ct == 6 ? 1472 : 256 * ct; }
  const int g = lane >> 4, l15 = lane & 15;

  if (wid >= 4) {
    const int lw = wid - 4;
    const unsigned xv = (lane >> 3) * 2560 + (((lane & 7) ^ ((4 * lw + (lane >> 4)) & 7)) << 4);
    const char* xg = (const char*)x + (size_t)(2 * (i0 + 8 * lw) + p) * 1280;
    const int ldw = isq ? NQKV * 2 : F_ * 2;
    unsigned wv[2];
#pragma unroll
    for (int e = 0; e < 2; e++) {
      int hi = lane >> 5, f = ((2 * lw + hi) & 3) | (e << 2);
      int c = (lane & 31) ^ (f << 1);
      int off = c * 16;
      if (isq) off = c < 10 ? c * 16 : (c == 10 || c == 31) ? 0 : c < 21 ? 1280 + (c - 11) * 16 : 2560 + (c - 21) * 16;
      wv[e] = hi * ldw + off;
    }
    const char* wg = (const char*)(isq ? wqkv : w1) + (size_t)(2 * lw) * ldw + colb * 2;
    auto issue = [&](int s, unsigned so) {
#pragma unroll
      for (int m = 0; m < 4; m++) dma16(L0 + so + (lw + 4 * m) * 1024, xv, xg + (size_t)m * (64 * 1280) + s * 128);
#pragma unroll
      for (int m = 0; m < 8; m++) dma16(L0 + so + 16384 + (lw + 4 * m) * 1024, wv[m & 1], wg + (size_t)(s * 64 + 8 * m) * ldw);
    };
    issue(0, 0);
    issue(1, STG);
    float sx = 0.f, sxx = 0.f, cs[4] = {0.f, 0.f, 0.f, 0.f};
    const int row = lw * 32 + (lane & 31);
    const unsigned xsb = L0 + row * 128;
    const unsigned xsw = (row >> 1) & 7;
    unsigned csb[4];
    {
      int q = l15 >> 2, pp = lane & 3, f = q | ((g & 1) << 2);
#pragma unroll
      for (int t = 0; t < 4; t++) csb[t] = L0 + 16384 + (8 * g + q) * 512 + ((128 * lw + 32 * t + 8 * pp) ^ (f << 5));
    }
    unsigned so = 0;
#pragma unroll 1
    for (int s = 0; s < 10; s++) {
      if (s < 9) asm volatile("s_waitcnt vmcnt(12)" ::: "memory"); else asm volatile("s_waitcnt vmcnt(0)" ::: "memory");
      BAR();
      unsigned s2 = so + 2 * STG; if (s2 >= 3 * STG) s2 -= 3 * STG;
      if (s < 8) issue(s + 2, s2);
#pragma unroll
      for (int i = 0; i < 4; i++) {
        u32x4 v = ldsread128(xsb + so + ((((lane >> 5) * 4 + i) ^ xsw) << 4));
#pragma unroll
        for (int e = 0; e < 4; e++) { sx = dot2(v[e], 0x3f803f80u, sx); sxx = dot2(v[e], v[e], sxx); }
      }
#pragma unroll
      for (int t = 0; t < 4; t++)
#pragma unroll
        for (int r = 0; r < 4; r++) {
          u32x2 v = trread(csb[t] + so + (r >> 1) * 16384 + (r & 1) * 2048);
          cs[t] = dot2(v[0], 0x3f803f80u, cs[t]); cs[t] = dot2(v[1], 0x3f803f80u, cs[t]);
        }
      so += STG; if (so == 3 * STG) so = 0;
    }
    {
      auto r = __builtin_amdgcn_permlane32_swap(__float_as_uint(sx), __float_as_uint(sx), false, false);
      sx = __uint_as_float(r[0]) + __uint_as_float(r[1]);
      auto r2 = __builtin_amdgcn_permlane32_swap(__float_as_uint(sxx), __float_as_uint(sxx), false, false);
      sxx = __uint_as_float(r2[0]) + __uint_as_float(r2[1]);
      float mean = sx * (1.f / 640.f);
      float var = fmaxf(sxx * (1.f / 640.f) - mean * mean, 0.f);
      float rstd = __builtin_amdgcn_rsqf(var + 1e-5f);
      *(LDS float*)(L0 + OFF_ROW + (lane >> 5) * 512 + row * 4) = (lane >> 5) ? rstd * mean : rstd;
#pragma unroll
      for (int t = 0; t < 4; t++) cs[t] = xsum(cs[t]);
      float c = g == 0 ? cs[0] : g == 1 ? cs[1] : g == 2 ? cs[2] : cs[3];
      *(LDS float*)(L0 + OFF_CS + (64 * lw + lane) * 4) = c;
    }
    BAR();
    if (isq) BAR();
    return;
  }

  const int wm = wid & 1, wn = wid >> 1;
  if (!isq) {
    { int c = wid * 64 + lane; *(LDS float*)(L0 + OFF_BIAS + c * 4) = bf2f(b1[colb + c]); }
    int u = ((14 * qd + (j - 18)) * 2 + p) * 4 + wid;
    if (u < 370) {
      int ks = u / 5, cg = u - ks * 5;
      const char* src = (const char*)(ks < 20 ? wo : w2) + cg * 256;
      unsigned sc = L0 + 2 * STG + wid * 8192;
      unsigned v = (lane >> 4) * 1280 + (lane & 15) * 16;
#pragma unroll
      for (int i = 0; i < 8; i++) {
        int t = i >> 2, gg = i & 3;
        int rb = ks < 16 ? 40 * ks + 20 * t + 4 * gg : ks < 20 ? 40 * (4 * (ks - 16) + gg) + 16 + 20 * t : 32 * (ks - 20) + 16 * t + 4 * gg;
        dma16(sc + i * 1024, v, src + (size_t)rb * 1280);
      }
      asm volatile("s_waitcnt vmcnt(0)" ::: "memory");
      unsigned ra = sc + (g * 4 + (l15 >> 2)) * 256 + (lane & 3) * 8;
#pragma unroll
      for (int nt = 0; nt < 8; nt++) {
        u32x2 lo = trread(ra + nt * 32), hi = trread(ra + nt * 32 + 4096);
        int NT = 8 * cg + nt, cb = NT / 5, tl = NT - cb * 5;
        *(u32x4*)(W2t + (size_t)((cb * NKS + ks) * 5 + tl) * 1024 + lane * 16) = u32x4{lo[0], lo[1], hi[0], hi[1]};
      }
    }
  }
  unsigned cell[8];
  unsigned cellg[8];
  {
    auto cellOff = [&](int nt, int c) -> unsigned {
      if (!isq) return 256 * wn + 32 * nt + 8 * c;
      int a = 80 * wn;
      switch (nt) {
        case 0: return a + 8 * c;
        case 1: return a + 40 + 8 * c;
        case 2: return 176 + a + 8 * c;
        case 3: return 176 + a + 40 + 8 * c;
        case 4: return (c >> 1) * 176 + a + 32 + (c & 1) * 40;
        case 5: return 336 + a + 8 * c;
        case 6: return 336 + a + 40 + 8 * c;
        default: return 336 + a + 32 + (c & 1) * 40;
      }
    };
#pragma unroll
    for (int nt = 0; nt < 8; nt++) { cell[nt] = cellOff(nt, lane & 3); cellg[nt] = cellOff(nt, (isq && nt >= 5) ? (l15 >> 2) : g); }
  }
  unsigned wb[8];
  {
    int q = l15 >> 2, f = q | ((g & 1) << 2);
#pragma unroll
    for (int nt = 0; nt < 8; nt++) wb[nt] = L0 + (8 * g + q) * 512 + (cell[nt] ^ (f << 5));
  }
  const unsigned xrow = L0 + (64 * wm + l15) * 128;
  const unsigned xb0 = xrow + ((g ^ (l15 >> 1)) << 4), xb1 = xrow + (((4 + g) ^ (l15 >> 1)) << 4);
  f32x4 acc[8][4];
#pragma unroll
  for (int nt = 0; nt < 8; nt++)
#pragma unroll
    for (int tb = 0; tb < 4; tb++) acc[nt][tb] = f32x4{0.f, 0.f, 0.f, 0.f};

  if (!isq) {
    mainloop<false>(acc, wb, xb0, xb1);
    float rs[4], rm[4];
#pragma unroll
    for (int tb = 0; tb < 4; tb++) {
      rs[tb] = *(LDS float*)(L0 + OFF_ROW + (64 * wm + 16 * tb + l15) * 4);
      rm[tb] = *(LDS float*)(L0 + OFF_ROW + 512 + (64 * wm + 16 * tb + l15) * 4);
    }
    const int rb = 2 * rt + wm;
    const int ks0 = 20 + (colb >> 5) + 4 * wn;
    const int skip = (colb == 1472 && wn == 0) ? 2 : 0;
    char* dst = Aws + (size_t)((p * 16 + rb) * NKS + ks0) * 4096 + lane * 16;
#pragma unroll
    for (int pr = 0; pr < 4; pr++) {
      f32x4 c0 = ldsreadf4(L0 + OFF_CS + cellg[2 * pr] * 2), c1 = ldsreadf4(L0 + OFF_CS + cellg[2 * pr + 1] * 2);
      f32x4 b0 = ldsreadf4(L0 + OFF_BIAS + cellg[2 * pr] * 2), b1v = ldsreadf4(L0 + OFF_BIAS + cellg[2 * pr + 1] * 2);
#pragma unroll
      for (int tb = 0; tb < 4; tb++) {
        float h[8];
#pragma unroll
        for (int r = 0; r < 4; r++) {
          h[r] = fmaf(rs[tb], acc[2 * pr][tb][r], fmaf(-rm[tb], c0[r], b0[r]));
          h[4 + r] = fmaf(rs[tb], acc[2 * pr + 1][tb][r], fmaf(-rm[tb], c1[r], b1v[r]));
        }
#pragma unroll
        for (int r = 0; r < 8; r++) {
          float v = h[r];
          float a = v * fmaf(v * v, -0.10294324f, -2.30220820f);
          float e = __builtin_amdgcn_exp2f(a);
          h[r] = v * __builtin_amdgcn_rcpf(1.f + e);
        }
        if (pr >= skip) *(u32x4*)(dst + pr * 4096 + tb * 1024) = u32x4{pk(h[0], h[1]), pk(h[2], h[3]), pk(h[4], h[5]), pk(h[6], h[7])};
      }
    }
  } else {
    mainloop<true>(acc, wb, xb0, xb1);
    const float qsc = 0.22360679775f * 1.44269504089f;
    u32x2 T[8][5];
    {
      f32x4 c[5];
#pragma unroll
      for (int nt = 0; nt < 5; nt++) c[nt] = ldsreadf4(L0 + OFF_CS + cellg[nt] * 2);
      float cv[3];
#pragma unroll
      for (int nt = 5; nt < 8; nt++) cv[nt - 5] = *(LDS float*)(L0 + OFF_CS + cellg[nt] * 2 + (lane & 3) * 4);
#pragma unroll
      for (int tb = 0; tb < 4; tb++) {
        float rs = *(LDS float*)(L0 + OFF_ROW + (64 * wm + 16 * tb + l15) * 4);
        float rm = *(LDS float*)(L0 + OFF_ROW + 512 + (64 * wm + 16 * tb + l15) * 4);
        f32x4 rs4 = ldsreadf4(L0 + OFF_ROW + (64 * wm + 16 * tb + 4 * g) * 4);
        f32x4 rm4 = ldsreadf4(L0 + OFF_ROW + 512 + (64 * wm + 16 * tb + 4 * g) * 4);
        float xs = g < 2 ? qsc : 1.f;
#pragma unroll
        for (int nt = 0; nt < 5; nt++) {
          float a = nt < 2 ? rs * qsc : nt < 4 ? rs : rs * xs;
          float m = nt < 2 ? rm * qsc : nt < 4 ? rm : rm * xs;
          float v[4];
#pragma unroll
          for (int r = 0; r < 4; r++) v[r] = fmaf(a, acc[nt][tb][r], -m * c[nt][r]);
          T[nt][tb + 1] = u32x2{pk(v[0], v[1]), pk(v[2], v[3])};
        }
#pragma unroll
        for (int nt = 5; nt < 8; nt++) {
          float v[4];
#pragma unroll
          for (int r = 0; r < 4; r++) v[r] = fmaf(rs4[r], acc[nt][tb][r], -rm4[r] * cv[nt - 5]);
          T[nt][tb + 1] = u32x2{pk(v[0], v[1]), pk(v[2], v[3])};
        }
      }
    }
    u32x2 XK[5];
#pragma unroll
    for (int tb = 1; tb < 5; tb++) {
#pragma unroll
      for (int e = 0; e < 2; e++) {
        auto r = __builtin_amdgcn_permlane32_swap(T[4][tb][e], T[4][tb][e], false, false);
        XK[tb][e] = r[1];
      }
    }
    {
      unsigned ex = L0 + wn * 3072 + lane * 16;
      if (wm == 0) {
        *(LDS u32x4*)(ex) = u32x4{T[2][4][0], T[2][4][1], T[3][4][0], T[3][4][1]};
        *(LDS u32x4*)(ex + 1024) = u32x4{XK[4][0], XK[4][1], T[5][4][0], T[5][4][1]};
        *(LDS u32x4*)(ex + 2048) = u32x4{T[6][4][0], T[6][4][1], T[7][4][0], T[7][4][1]};
      }
      BAR();
      u32x4 a = ldsread128(ex), bq = ldsread128(ex + 1024), c = ldsread128(ex + 2048);
      T[2][0] = u32x2{a[0], a[1]}; T[3][0] = u32x2{a[2], a[3]};
      XK[0] = u32x2{bq[0], bq[1]}; T[5][0] = u32x2{bq[2], bq[3]};
      T[6][0] = u32x2{c[0], c[1]}; T[7][0] = u32x2{c[2], c[3]};
    }
    f32x4 mP, mC; unsigned sel[2];
    {
      const float NEG = -1e30f;
#pragma unroll
      for (int r = 0; r < 4; r++) {
        int jj = 4 * g + r;
        mC[r] = (jj <= l15 && jj >= l15 - 7) ? 0.f : NEG;
        mP[r] = (jj >= l15 + 9) ? 0.f : NEG;
      }
#pragma unroll
      for (int e = 0; e < 2; e++) sel[e] = ((4 * g + 2 * e > l15) ? 0xffffu : 0u) | ((4 * g + 2 * e + 1 > l15) ? 0xffff0000u : 0u);
    }
    const unsigned gm0 = g == 0 ? 0xffffffffu : 0u, gm1 = g == 1 ? 0xffffffffu : 0u;
    const u32x4 ones = {0x3f803f80u, 0x3f803f80u, 0x3f803f80u, 0x3f803f80u};
    const f32x4 z4 = {0.f, 0.f, 0.f, 0.f};
    const int hp = 2 * hg + wn;
#pragma unroll
    for (int tb = 0; tb < 4; tb++) {
      const bool first = (tb == 0 && wm == 0);
      if (first && rt != 0) continue;
      f32x4 mPP = mP;
      if (first) mPP = f32x4{-1e30f, -1e30f, -1e30f, -1e30f};
      u32x2 o[2], o2[2];
#pragma unroll
      for (int h = 0; h < 2; h++) {
        unsigned gm = h ? gm1 : gm0;
        u32x4 Q = {T[h][tb + 1][0], T[h][tb + 1][1], T[4][tb + 1][0] & gm, T[4][tb + 1][1] & gm};
        u32x4 Kp = {T[2 + h][tb][0], T[2 + h][tb][1], XK[tb][0], XK[tb][1]};
        u32x4 Kc = {T[2 + h][tb + 1][0], T[2 + h][tb + 1][1], XK[tb + 1][0], XK[tb + 1][1]};
        f32x4 sp = mfma(Kp, Q, mPP), sc = mfma(Kc, Q, mC);
        float s0 = fmaxf(sp[0], sc[0]), s1 = fmaxf(sp[1], sc[1]), s2 = fmaxf(sp[2], sc[2]), s3 = fmaxf(sp[3], sc[3]);
        float m = xmax(fmaxf(fmaxf(s0, s1), fmaxf(s2, s3)));
        unsigned p0 = pk(__builtin_amdgcn_exp2f(s0 - m), __builtin_amdgcn_exp2f(s1 - m));
        unsigned p1 = pk(__builtin_amdgcn_exp2f(s2 - m), __builtin_amdgcn_exp2f(s3 - m));
        u32x4 P = {p0 & sel[0], p1 & sel[1], p0 & ~sel[0], p1 & ~sel[1]};
        u32x4 V = {T[5 + h][tb][0], T[5 + h][tb][1], T[5 + h][tb + 1][0], T[5 + h][tb + 1][1]};
        u32x4 V2 = {T[7][tb][0], T[7][tb][1], T[7][tb + 1][0], T[7][tb + 1][1]};
        f32x4 sum = mfma(ones, P, z4), oa = mfma(V, P, z4), ob = mfma(V2, P, z4);
        float ri = __builtin_amdgcn_rcpf(sum[0]);
        o[h] = u32x2{pk(oa[0] * ri, oa[1] * ri), pk(oa[2] * ri, oa[3] * ri)};
        o2[h] = u32x2{pk(ob[0] * ri, ob[1] * ri), pk(ob[2] * ri, ob[3] * ri)};
      }
      const int B = 7 * rt + 4 * wm + tb;
      char* dst = Aws + (size_t)((p * 16 + (B >> 2)) * NKS) * 4096 + (B & 3) * 1024;
      *(u32x4*)(dst + hp * 4096 + lane * 16) = u32x4{o[0][0], o[0][1], o[1][0], o[1][1]};
      auto r0 = __builtin_amdgcn_permlane16_swap(o2[1][0], o2[1][0], false, false);
      auto r1 = __builtin_amdgcn_permlane16_swap(o2[1][1], o2[1][1], false, false);
      if (g == 0) *(u32x4*)(dst + (16 + (hp >> 2)) * 4096 + (hp & 3) * 256 + lane * 16) = u32x4{o2[0][0], o2[0][1], r0[1], r1[1]};
    }
  }
}

__global__ __launch_bounds__(512, 2) void k2(const u16* __restrict__ x, const u16* __restrict__ b2, const char* __restrict__ Aws,
                                             const char* __restrict__ W2t, u16* __restrict__ out) {
  extern __shared__ __attribute__((aligned(16))) char smem[];
  const unsigned L0 = (unsigned)(size_t)(LDS char*)smem;
  const int tid = threadIdx.x, lane = tid & 63;
  const int wid = __builtin_amdgcn_readfirstlane(tid >> 6);
  const int b = blockIdx.x, xcd = b & 7, j = b >> 3, p = xcd & 1, qd = xcd >> 1;
  const int rb = 4 * qd + (j >> 3), cb = j & 7;
  const int ks0 = wid < 2 ? 10 * wid : 20 + 9 * (wid - 2);
  auto ar = __builtin_amdgcn_make_buffer_rsrc((void*)(Aws + (size_t)((p * 16 + rb) * NKS + ks0) * 4096), 0, (wid < 2 ? 10 : 9) * 4096, 0x00020000);
  auto wr = __builtin_amdgcn_make_buffer_rsrc((void*)(W2t + (size_t)(cb * NKS + ks0) * 5120), 0, (wid < 2 ? 10 : 9) * 5120, 0x00020000);
  const int lo = lane * 16;
  constexpr int PD = 4;
  u32x4 af[PD][4], wf[PD][5];
  auto ld = [&](int s) {
#pragma unroll
    for (int i = 0; i < 5; i++) wf[s % PD][i] = __builtin_amdgcn_raw_buffer_load_b128(wr, lo + (i & 3) * 1024, s * 5120 + (i >> 2) * 4096, 0);
#pragma unroll
    for (int i = 0; i < 4; i++) af[s % PD][i] = __builtin_amdgcn_raw_buffer_load_b128(ar, lo + i * 1024, s * 4096, 0);
  };
  f32x4 acc[5][4];
#pragma unroll
  for (int i = 0; i < 5; i++)
#pragma unroll
    for (int t = 0; t < 4; t++) acc[i][t] = f32x4{0.f, 0.f, 0.f, 0.f};
#pragma unroll
  for (int s = 0; s < PD; s++) ld(s);
  SB();
#pragma unroll
  for (int s = 0; s < 9; s++) {
#pragma unroll
    for (int i = 0; i < 5; i++)
#pragma unroll
      for (int t = 0; t < 4; t++) acc[i][t] = mfma(wf[s % PD][i], af[s % PD][t], acc[i][t]);
    SB();
    if (s + PD < 10) ld(s + PD);
    SB();
  }
  if (wid < 2) {
#pragma unroll
    for (int i = 0; i < 5; i++)
#pragma unroll
      for (int t = 0; t < 4; t++) acc[i][t] = mfma(wf[9 % PD][i], af[9 % PD][t], acc[i][t]);
  }
#pragma unroll
  for (int i = 0; i < 5; i++)
#pragma unroll
    for (int t = 0; t < 4; t++) *(LDS f32x4*)(L0 + wid * 20480 + (t * 5 + i) * 1024 + lane * 16) = acc[i][t];
  BAR();
#pragma unroll
  for (int r = 0; r < 3; r++) {
    int it = r * 512 + tid;
    if (it < 1280) {
      int tile = it >> 6, t = tile / 5, i = tile - t * 5;
      unsigned a = L0 + it * 16;
      f32x4 s = ldsreadf4(a) + ldsreadf4(a + 20480);
      f32x4 y = (ldsreadf4(a + 2 * 20480) + ldsreadf4(a + 3 * 20480)) + (ldsreadf4(a + 4 * 20480) + ldsreadf4(a + 5 * 20480)) +
                (ldsreadf4(a + 6 * 20480) + ldsreadf4(a + 7 * 20480));
      int col = 80 * cb + 16 * i + 4 * (lane >> 4);
      int tok = 2 * (64 * rb + 16 * t + (lane & 15)) + p;
      u32x2 xv = *(const u32x2*)(x + (size_t)tok * D_ + col);
      u32x2 bv = *(const u32x2*)(b2 + col);
      float o[4];
#pragma unroll
      for (int e = 0; e < 4; e++) {
        float xx = __uint_as_float((e & 1) ? (xv[e >> 1] & 0xffff0000u) : (xv[e >> 1] << 16));
        float bb = __uint_as_float((e & 1) ? (bv[e >> 1] & 0xffff0000u) : (bv[e >> 1] << 16));
        float yy = y[e] + bb;
        float gt = yy * __builtin_amdgcn_rcpf(1.f + __builtin_amdgcn_exp2f(-1.44269504089f * yy));
        o[e] = fmaf(s[e], gt, xx);
      }
      *(u32x2*)(out + (size_t)tok * D_ + col) = u32x2{pk(o[0], o[1]), pk(o[2], o[3])};
    }
  }
}

extern "C" void kernel_launch(void* const* d_in, const int* in_sizes, int n_in, void* d_out, int out_size, void* d_ws, size_t ws_size,
                              hipStream_t stream) {
  const u16* x = (const u16*)d_in[0]; const u16* wqkv = (const u16*)d_in[1]; const u16* wo = (const u16*)d_in[2];
  const u16* w1 = (const u16*)d_in[4]; const u16* b1 = (const u16*)d_in[5]; const u16* w2 = (const u16*)d_in[6]; const u16* b2 = (const u16*)d_in[7];
  char* Aws = (char*)d_ws; char* W2t = Aws + (size_t)32 * NKS * 4096;
  (void)hipFuncSetAttribute((const void*)k1, hipFuncAttributeMaxDynamicSharedMemorySize, LDS1);
  (void)hipFuncSetAttribute((const void*)k2, hipFuncAttributeMaxDynamicSharedMemorySize, 163840);
  k1<<<256, 512, LDS1, stream>>>(x, wqkv, w1, b1, wo, w2, Aws, W2t);
  k2<<<256, 512, 163840, stream>>>(x, b2, Aws, W2t, (u16*)d_out);
}
```

```cpp
#define .cpp e1.cpp -DOUT_SC1
#include <hip/hip_runtime.h>
#include <hip/hip_bf16.h>
#pragma clang diagnostic ignored "-Wint-to-pointer-cast"
typedef unsigned short u16;
typedef unsigned long long u64;
typedef short s16x4 __attribute__((ext_vector_type(4)));
typedef __bf16 bf16x2 __attribute__((ext_vector_type(2)));
typedef __bf16 bf16x8 __attribute__((ext_vector_type(8)));
typedef float f32x4 __attribute__((ext_vector_type(4)));
typedef float f32x2 __attribute__((ext_vector_type(2)));
typedef unsigned u32x2 __attribute__((ext_vector_type(2)));
typedef unsigned u32x4 __attribute__((ext_vector_type(4)));
typedef __attribute__((address_space(1))) u64 gu64;
#define LDS __attribute__((address_space(3)))
#define DEV __device__ __forceinline__
#define RLX_AGENT __ATOMIC_RELAXED, __HIP_MEMORY_SCOPE_AGENT

constexpr int D_ = 640, F_ = 1728, NQKV = 1920;
constexpr int STG = 49152;
constexpr int NKS = 74;
constexpr int OFF_ROW = 153600;
constexpr int OFF_CS = OFF_ROW + 1024;
constexpr int OFF_EX = OFF_CS + 2048;
constexpr int OFF_TAG = OFF_EX + 6144;
constexpr int LDS_BYTES = 163840;
constexpr unsigned WS_A = 0;
constexpr unsigned WS_W = 32 * NKS * 4096;
constexpr unsigned WS_C = WS_W + 8 * NKS * 5120;
constexpr unsigned WS_FW = WS_C + 256, WS_FH = WS_C + 2048, WS_FQ = WS_C + 8192, WS_G = WS_C + 16384, WS_END = WS_G + 32768;
#define AAUX 16
constexpr int SPIN_CAP = 1 << 20;

DEV void dma16(unsigned lds, unsigned voff, const void* base) {
  asm volatile("s_mov_b32 m0, %0\n\ts_nop 0\n\tglobal_load_lds_dwordx4 %1, %2" ::"s"(lds), "v"(voff), "s"(base) : "memory");
}
DEV void dma16c(unsigned lds, unsigned voff, const void* base) {
  asm volatile("s_mov_b32 m0, %0\n\ts_nop 0\n\tglobal_load_lds_dwordx4 %1, %2 sc1" ::"s"(lds), "v"(voff), "s"(base) : "memory");
}
DEV f32x4 mfma(u32x4 a, u32x4 b, f32x4 c) {
  return __builtin_amdgcn_mfma_f32_16x16x32_bf16(__builtin_bit_cast(bf16x8, a), __builtin_bit_cast(bf16x8, b), c, 0, 0, 0);
}
DEV u32x2 trread(unsigned addr) {
  return __builtin_bit_cast(u32x2, __builtin_amdgcn_ds_read_tr16_b64_v4i16((LDS s16x4*)addr));
}
DEV u32x4 ldsread128(unsigned addr) { return *(LDS u32x4*)addr; }
DEV f32x4 ldsreadf4(unsigned addr) { return *(LDS f32x4*)addr; }
DEV unsigned pk(float a, float b) { f32x2 v = {a, b}; return __builtin_bit_cast(unsigned, __builtin_convertvector(v, bf16x2)); }
DEV float dot2(unsigned a, unsigned b, float c) {
  return __builtin_amdgcn_fdot2_f32_bf16(__builtin_bit_cast(bf16x2, a), __builtin_bit_cast(bf16x2, b), c, false);
}
DEV float bf2f(u16 v) { return __uint_as_float(((unsigned)v) << 16); }
#define SB() __builtin_amdgcn_sched_barrier(0)
#define BAR() asm volatile("s_waitcnt lgkmcnt(0)\n\ts_barrier" ::: "memory")
#define VMCNT(n) asm volatile("s_waitcnt vmcnt(" #n ")" ::: "memory")
#define ST_INIT()
#define STAMP(i)
#define ST_END(base, kern)
#define ST_ARG
#define ST_PASS
DEV float xmax(float v) {
  unsigned u = __float_as_uint(v);
  auto r = __builtin_amdgcn_permlane32_swap(u, u, false, false);
  v = fmaxf(__uint_as_float(r[0]), __uint_as_float(r[1]));
  u = __float_as_uint(v);
  auto r2 = __builtin_amdgcn_permlane16_swap(u, u, false, false);
  return fmaxf(__uint_as_float(r2[0]), __uint_as_float(r2[1]));
}
template <int N> DEV float ror16(float v) {
  return __uint_as_float(__builtin_amdgcn_update_dpp(0u, __float_as_uint(v), 0x120 + N, 0xf, 0xf, false));
}
DEV float sum16(float v) { v += ror16<8>(v); v += ror16<4>(v); v += ror16<2>(v); v += ror16<1>(v); return v; }
DEV u64 ld_flag(const char* ws, unsigned off) { return __hip_atomic_load((gu64*)(ws + off), RLX_AGENT); }
DEV void st_flag(char* ws, unsigned off, u64 v) { __hip_atomic_store((gu64*)(ws + off), v, RLX_AGENT); }
DEV void wait_flags(const char* ws, unsigned off, bool need, u64 tag) {
#pragma unroll 1
  for (int it = 0; it < SPIN_CAP; it++) {
    u64 v = ld_flag(ws, off);
    if (__all(!need || v == tag)) break;
    __builtin_amdgcn_s_sleep(1);
  }
  asm volatile("" ::: "memory");
}

template <bool QKV>
DEV void mainloop(f32x4 (&acc)[8][4], f32x4 (&cs)[8], const unsigned (&wb)[8], unsigned xb0, unsigned xb1, unsigned wsw, unsigned csw,
                  bool cswr ST_ARG) {
  u32x2 wl[4], wh[4];
  u32x4 xf[2][4];
  u32x4 ones = {0x3f803f80u, 0x3f803f80u, 0x3f803f80u, 0x3f803f80u};
  asm volatile("" : "+v"(ones));
#pragma unroll
  for (int nt = 0; nt < 8; nt++) {
    asm volatile("" : "+v"(cs[nt]));
#pragma unroll
    for (int tb = 0; tb < 4; tb++) asm volatile("" : "+v"(acc[nt][tb]));
  }
  __builtin_amdgcn_s_waitcnt(0xC07F);
  STAMP(2);
  BAR();
  STAMP(3);
  SB();
  wl[0] = trread(wb[0]); wh[0] = trread(wb[0] + 2048);
#pragma unroll
  for (int tb = 0; tb < 4; tb++) xf[0][tb] = ldsread128(xb0 + tb * 2048);
  wl[1] = trread(wb[1]); wh[1] = trread(wb[1] + 2048);
  unsigned so = 0;
  unsigned ad = wb[2], ad2 = wb[3], xa = xb1;
#pragma unroll 1
  for (int s = 0; s < 10; s++) {
    unsigned sn = so + STG; if (sn == 3 * STG) sn = 0;
    const unsigned so1 = so + wsw;
#pragma unroll
    for (int ts = 0; ts < 16; ts++) {
      const int nt = ts & 7;
      const u32x4 w = {wl[ts & 3][0], wl[ts & 3][1], wh[ts & 3][0], wh[ts & 3][1]};
      auto M = [&](int tb) {
        SB();
        if (QKV && nt >= 5) acc[nt][tb] = mfma(xf[ts >> 3][tb], w, acc[nt][tb]);
        else acc[nt][tb] = mfma(w, xf[ts >> 3][tb], acc[nt][tb]);
        SB();
      };
      auto MC = [&]() {
        SB();
        if (QKV && nt >= 5) cs[nt] = mfma(ones, w, cs[nt]);
        else cs[nt] = mfma(w, ones, cs[nt]);
        SB();
      };
      auto nextad = [&](int t) {
        ad = wb[t & 7] + (t >= 16 ? sn : t >= 8 ? so1 : so);
        asm volatile("" : "+v"(ad));
      };
      if (ts == 0) {
        M(0); wl[2] = trread(ad); M(1); wh[2] = trread(ad + 2048); M(2); wl[3] = trread(ad2); M(3); wh[3] = trread(ad2 + 2048); MC(); nextad(4);
      } else if (ts <= 12) {
        M(0); wl[(ts + 3) & 3] = trread(ad); M(1); wh[(ts + 3) & 3] = trread(ad + 2048); M(2);
        if (ts >= 2 && ts < 6) xf[1][ts - 2] = ldsread128(xa + (ts - 2) * 2048);
        M(3);
        if (ts < 8) MC();
        nextad(ts + 4);
      } else if (ts == 13) {
        M(0);
        xa = xb0 + sn; asm volatile("" : "+v"(xa));
        M(1);
        ad2 = wb[1] + sn; asm volatile("" : "+v"(ad2));
        M(2); M(3);
        if (s == 9) {
#pragma unroll
          for (int i = 0; i < 8; i++) {
            if (QKV && i >= 5) { if ((threadIdx.x & 48) == 0) *(LDS float*)(csw + i * 64 + (threadIdx.x & 15) * 4 - (threadIdx.x & 48)) = cs[i][0]; }
            else if (cswr) *(LDS f32x4*)(csw + i * 64) = cs[i];
          }
        }
        SB();
        BAR();
      } else if (ts == 14) {
        M(0); wl[0] = trread(ad); M(1); wh[0] = trread(ad + 2048); M(2); xf[0][0] = ldsread128(xa); M(3); xf[0][1] = ldsread128(xa + 2048);
      } else {
        M(0); xf[0][2] = ldsread128(xa + 4096); M(1); xf[0][3] = ldsread128(xa + 6144); M(2); wl[1] = trread(ad2);
        nextad(18);
        M(3); wh[1] = trread(ad2 + 2048);
        SB();
        ad2 = wb[3] + sn; asm volatile("" : "+v"(ad2));
        xa = xb1 + sn; asm volatile("" : "+v"(xa));
      }
    }
    so = sn;
  }
  SB();
#pragma unroll
  for (int nt = 0; nt < 8; nt++) {
    if (QKV && nt >= 5) cs[nt][0] += *(LDS float*)((csw ^ 512) + nt * 64 + (threadIdx.x & 15) * 4 - (threadIdx.x & 48));
    else cs[nt] += ldsreadf4((csw ^ 512) + nt * 64);
  }
}

struct P2 { int p, rb, cb, ks0; };
DEV int kstep(const P2& t, int ks0, int s) { int k = s + t.cb; return ks0 + (k >= 9 ? k - 9 : k); }

template <typename R>
DEV void ldW(u32x4 (&w)[5], R rs, int lo, unsigned soff) {
#pragma unroll
  for (int i = 0; i < 5; i++) w[i] = __builtin_amdgcn_raw_buffer_load_b128(rs, lo + (i & 3) * 1024, soff + (i >> 2) * 4096, 16);
}
template <typename R>
DEV void st16(R rs, unsigned off, u32x4 v) { __builtin_amdgcn_raw_buffer_store_b128(v, rs, off, 0, 16); }

template <typename R>
DEV void phase2(u32x4 (&wr)[5][5], R rs, const char* ws, const u16* x, const u16* b2, u16* out, P2 t, int wid, int lane, u64 tag ST_ARG) {
  const int tid = wid * 64 + lane;
  const int lo = lane * 16;
  u32x2 xv[3], bv[3];
  const bool sw = (wid | 1) == 5;
  {
    unsigned off; bool need;
    if (sw) {
      int B = 4 * t.rb + ((lane & 16) ? 3 : 0);
      int rt = B == 0 ? 0 : (B - 1) / 7;
      int G = 2 * rt + ((B - 7 * rt) >= 4);
      off = WS_FQ + ((t.p * 18 + G) * 16 + (lane & 15)) * 8; need = true;
    } else {
      int c = lane & 15, f0 = (t.ks0 - 20) * 32;
      int lo_c = c < 13 ? 128 * c : 1600, hi_c = c < 12 ? 128 * c + 128 : c == 12 ? 1600 : 1728;
      off = WS_FH + ((t.p * 16 + t.rb) * 16 + c) * 8; need = c < 14 && lo_c < f0 + 288 && hi_c > f0;
    }
    wait_flags(ws, off, need, tag);
  }
  STAMP(21);
  const unsigned abase = WS_A + (t.p * 16 + t.rb) * NKS * 4096;
  u32x4 af[10][4];
  auto ldA = [&](int s) {
    unsigned so = s < 9 ? abase + kstep(t, t.ks0, s) * 4096 : sw ? abase + (t.ks0 + 9) * 4096 : WS_END;
#pragma unroll
    for (int i = 0; i < 4; i++) af[s][i] = __builtin_amdgcn_raw_buffer_load_b128(rs, lo + i * 1024, so, AAUX);
    SB();
  };
  f32x4 acc[5][4];
  ldA(0); ldA(1); ldA(2);
  const unsigned reg = wid * 20480 + lo;
  auto ldL = [&](int s) {
#pragma unroll
    for (int i = 0; i < 5; i++) wr[s - 5][i] = ldsread128(reg + (s - 5) * 5120 + i * 1024);
  };
#pragma unroll
  for (int s = 0; s < 10; s++) {
    if (s < 9 || sw) {
#pragma unroll
      for (int tb = 0; tb < 4; tb++)
#pragma unroll
        for (int i = 0; i < 5; i++)
          acc[i][tb] = mfma(wr[s % 5][i], af[s][tb], s == 0 ? f32x4{0.f, 0.f, 0.f, 0.f} : acc[i][tb]);
    }
    SB();
    if (s < 3) { ldA(2 * s + 3); ldA(2 * s + 4); }
    else if (s == 3) { ldA(9); ldL(5); }
    else if (s == 4) { ldW(wr[4], rs, lo, sw ? WS_W + (t.cb * NKS + t.ks0 + 9) * 5120 : WS_END); ldL(6); }
    else if (s == 5) {
      ldL(7);
#pragma unroll
      for (int r = 0; r < 3; r++) {
        int it = r * 512 + tid; it = it < 1280 ? it : tid;
        int tile = it >> 6, tb = tile / 5, i = tile - tb * 5;
        int col = 80 * t.cb + 16 * i + 4 * (lane >> 4);
        int tok = 2 * (64 * t.rb + 16 * tb + (lane & 15)) + t.p;
        xv[r] = *(const u32x2*)(x + (size_t)tok * D_ + col);
        bv[r] = *(const u32x2*)(b2 + col);
      }
    } else if (s == 6) ldL(8);
    SB();
  }
  STAMP(22);
#pragma unroll
  for (int i = 0; i < 5; i++)
#pragma unroll
    for (int tb = 0; tb < 4; tb++) *(LDS f32x4*)(reg + (tb * 5 + i) * 1024) = acc[i][tb];
  STAMP(23);
  BAR();
  STAMP(24);
#pragma unroll
  for (int r = 0; r < 3; r++) {
    int it = r * 512 + tid;
    if (it < 1280) {
      int tile = it >> 6, tb = tile / 5, i = tile - tb * 5;
      unsigned a = it * 16;
      f32x4 s = ldsreadf4(a + 4 * 20480) + ldsreadf4(a + 5 * 20480);
      f32x4 y = (ldsreadf4(a) + ldsreadf4(a + 20480)) + (ldsreadf4(a + 2 * 20480) + ldsreadf4(a + 3 * 20480)) +
                (ldsreadf4(a + 6 * 20480) + ldsreadf4(a + 7 * 20480));
      int col = 80 * t.cb + 16 * i + 4 * (lane >> 4);
      int tok = 2 * (64 * t.rb + 16 * tb + (lane & 15)) + t.p;
      float o[4];
#pragma unroll
      for (int e = 0; e < 4; e++) {
        float xx = __uint_as_float((e & 1) ? (xv[r][e >> 1] & 0xffff0000u) : (xv[r][e >> 1] << 16));
        float bb = __uint_as_float((e & 1) ? (bv[r][e >> 1] & 0xffff0000u) : (bv[r][e >> 1] << 16));
        float yy = y[e] + bb;
        float gt = yy * __builtin_amdgcn_rcpf(1.f + __builtin_amdgcn_exp2f(-1.44269504089f * yy));
        o[e] = fmaf(s[e], gt, xx);
      }
      *(u32x2*)(out + (size_t)tok * D_ + col) = u32x2{pk(o[0], o[1]), pk(o[2], o[3])};
    }
  }
  STAMP(25);
}

DEV f32x2 lo2(f32x4 v) { return __builtin_shufflevector(v, v, 0, 1); }
DEV f32x2 hi2(f32x4 v) { return __builtin_shufflevector(v, v, 2, 3); }
DEV f32x2 fma2(f32x2 a, f32x2 b, f32x2 c) { return __builtin_elementwise_fma(a, b, c); }
DEV f32x2 bc2(float a) { return f32x2{a, a}; }
DEV unsigned pk2(f32x2 v) { return __builtin_bit_cast(unsigned, __builtin_convertvector(v, bf16x2)); }

__global__ __launch_bounds__(512, 2) void fused(const u16* __restrict__ x, const u16* __restrict__ wqkv, const u16* __restrict__ w1,
                                                const u16* __restrict__ b1, const u16* __restrict__ wo, const u16* __restrict__ w2,
                                                const u16* __restrict__ b2, char* ws, u16* __restrict__ out) {
  extern __shared__ __attribute__((aligned(16))) char smem[];
  const int tid = threadIdx.x, lane = tid & 63;
  const int wid = __builtin_amdgcn_readfirstlane(tid >> 6);
  const int b = blockIdx.x, xcd = b & 7, j = b >> 3, p = xcd & 1, qd = xcd >> 1;
  const bool isq = j < 18;
  const int u1 = 14 * qd + (j - 18);
  const int hg = 2 * qd + (j >= 9), ct = u1 >> 3;
  const int rt = isq ? (j >= 9 ? j - 9 : j) : (u1 & 7);
  const int i0 = (isq ? 112 : 128) * rt;
  const int colb = isq ? 80 * hg : (ct == 6 ? 1472 : 256 * ct);
  const int g = lane >> 4, l15 = lane & 15;
  ST_INIT();
  auto rs = __builtin_amdgcn_make_buffer_rsrc((void*)ws, 0, WS_END, 0x00020000);
  P2 t2; t2.p = p; t2.rb = 4 * qd + (j >> 3); t2.cb = j & 7;
  t2.ks0 = (wid | 1) == 5 ? 10 * (wid - 4) : 20 + 9 * (wid < 4 ? wid : wid - 2);
  const unsigned wcol = WS_W + t2.cb * NKS * 5120;
  const int lo = lane * 16;

  if (__builtin_expect(wid >= 4, 1)) {
    const int lw = wid - 4;
    const unsigned xv = (lane >> 3) * 2560 + (((lane & 7) ^ ((4 * lw + (lane >> 4)) & 7)) << 4);
    const char* xg = (const char*)x + (size_t)(2 * (i0 + 8 * lw) + p) * 1280;
    const int ldw = isq ? NQKV * 2 : F_ * 2;
    unsigned wv[2];
#pragma unroll
    for (int e = 0; e < 2; e++) {
      int hi = lane >> 5, f = ((2 * lw + hi) & 3) | (e << 2);
      int c = (lane & 31) ^ (f << 1);
      int oq = c * 16 + (c > 10 ? 1280 - 176 : 0) + (c > 20 ? 1280 - 160 : 0);
      oq = (c == 10 || c == 31) ? 0 : oq;
      wv[e] = hi * ldw + (isq ? oq : c * 16);
    }
    const char* wg = (const char*)(isq ? wqkv : w1) + (size_t)(2 * lw) * ldw + colb * 2;
    auto issue = [&](int s, unsigned so) {
#pragma unroll
      for (int m = 0; m < 4; m++) dma16(so + (lw + 4 * m) * 1024, xv, xg + (size_t)m * (64 * 1280) + s * 128);
#pragma unroll
      for (int m = 0; m < 8; m++) dma16(so + 16384 + (lw + 4 * m) * 1024, wv[m & 1], wg + (size_t)(s * 64 + 8 * m) * ldw);
    };
    STAMP(4);
    issue(0, 0);
    STAMP(5);
    issue(1, STG);
    STAMP(2);
    const int w1id = u1 * 2 + p;
    const int u = w1id * 4 + lw;
    const bool rtile = !isq && u < 370;
    const int rks = u / 5, rcg = u - rks * 5;
    const char* rsrc_ = (const char*)(rks < 20 ? wo : w2) + rcg * 256 + (size_t)(rks < 16 ? 40 * rks : rks < 20 ? 160 * (rks - 16) + 16 : 32 * (rks - 20)) * 1280;
    const int rdt = (rks < 20 ? 20 : 16) * 1280, rdg = ((rks >= 16 && rks < 20) ? 40 : 4) * 1280;
    const unsigned rsc = 3 * STG + lw * 4096;
    const unsigned rv = g * 1280 + l15 * 16;
    const unsigned rra = rsc + (g * 4 + (l15 >> 2)) * 256 + (lane & 3) * 8;
    u32x2 rlo[8];
    VMCNT(12);
    STAMP(3);
    BAR();
    issue(2, 2 * STG);
    const u32x2 tg = *(LDS u32x2*)OFF_TAG;
    const unsigned t0 = __builtin_amdgcn_readfirstlane(tg[0]), t1 = __builtin_amdgcn_readfirstlane(tg[1]);
    const u64 tag = ((u64)t1 << 32) | t0;
    VMCNT(12);
    BAR();
    unsigned s2 = 0;
#pragma unroll 1
    for (int s = 1; s < 7; s++) {
      if (s <= 3) {
        if (rtile) {
          if (s == 2) {
#pragma unroll
            for (int nt = 0; nt < 8; nt++) rlo[nt] = trread(rra + nt * 32);
            asm volatile("s_waitcnt lgkmcnt(0)" ::: "memory");
          }
          if (s == 3) {
#pragma unroll
            for (int nt = 0; nt < 8; nt++) {
              u32x2 hi = trread(rra + nt * 32);
              int NT = 8 * rcg + nt, cb = (NT * 205) >> 10;
              st16(rs, lo + WS_W + ((cb * (NKS - 1) + rks) * 5 + NT) * 1024, u32x4{rlo[nt][0], rlo[nt][1], hi[0], hi[1]});
            }
          } else {
#pragma unroll
            for (int i = 0; i < 4; i++) dma16(rsc + i * 1024, rv, rsrc_ + (s - 1) * rdt + i * rdg);
          }
        }
      } else if (s == 4) {
        if (!isq && tid == 256) st_flag(ws, WS_FW + w1id * 8, tag);
      }
      issue(s + 2, s2);
      s2 += STG; if (s2 == 3 * STG) s2 = 0;
      VMCNT(12);
      BAR();
    }
    const unsigned goff = WS_G + (p * 1024 + i0 + lw * 32 + (lane & 31)) * 16;
    const unsigned foff = WS_FW + (lane < 56 ? lane : 0) * 16;
    const unsigned boff = ((isq ? 0 : colb) + lw * 64 + lane) * 2;
    u32x4 gr, fw; unsigned bias;
    asm volatile("global_load_dwordx4 %0, %3, %6 sc1\n\tglobal_load_dwordx4 %1, %4, %6 sc1\n\tglobal_load_ushort %2, %5, %7"
                 : "=&v"(gr), "=&v"(fw), "=&v"(bias) : "v"(goff), "v"(foff), "v"(boff), "s"(ws), "s"(b1) : "memory");
    SB();
    issue(9, s2);
    asm volatile("s_waitcnt vmcnt(12)" : "+v"(gr), "+v"(fw), "+v"(bias)::"memory");
    BAR();
    STAMP(6);
    {
#pragma unroll 1
      for (int it = 0; it < SPIN_CAP; it++) {
        if (__all(gr[1] == t0 && gr[3] == t1 && fw[0] == t0 && fw[1] == t1 && fw[2] == t0 && fw[3] == t1)) break;
        __builtin_amdgcn_s_sleep(1);
        asm volatile("global_load_dwordx4 %0, %2, %4 sc1\n\tglobal_load_dwordx4 %1, %3, %4 sc1\n\ts_waitcnt vmcnt(0)" : "=&v"(gr), "=&v"(fw) : "v"(goff), "v"(foff), "s"(ws) : "memory");
      }
      asm volatile("" ::: "memory");
      *(LDS unsigned*)(OFF_ROW + (lane >> 5) * 512 + (lw * 32 + (lane & 31)) * 4) = (lane >> 5) ? gr[2] : gr[0];
      *(LDS unsigned*)(OFF_EX + (lw * 64 + lane) * 4) = bias << 16;
      if (lane == 0) *(LDS u32x4*)OFF_TAG = u32x4{t0, t1, 0u, 0u};
    }
    STAMP(7);
    u32x4 wr[5][5];
    SB();
    ldW(wr[0], rs, lo, wcol + kstep(t2, t2.ks0, 0) * 5120); ldW(wr[1], rs, lo, wcol + kstep(t2, t2.ks0, 1) * 5120);
    SB();
    VMCNT(10);
    STAMP(8);
    BAR();
    SB();
    ldW(wr[2], rs, lo, wcol + kstep(t2, t2.ks0, 2) * 5120); ldW(wr[3], rs, lo, wcol + kstep(t2, t2.ks0, 3) * 5120);
    SB();
    STAMP(9);
    BAR();
    STAMP(16);
    SB();
    ldW(wr[4], rs, lo, wcol + kstep(t2, t2.ks0, 4) * 5120);
    SB();
    {
      auto fill = [&](int w, int ks0, int s) {
        const char* src = ws + wcol + kstep(t2, ks0, s) * 5120;
#pragma unroll
        for (int i = 0; i < 5; i++) dma16c(w * 20480 + (s - 5) * 5120 + i * 1024, lo, src + i * 1024);
      };
#pragma unroll 1
      for (int s = 5; s < 9; s++) fill(lw, 20 + 9 * lw, s);
      const int n = lw == 3 ? 7 : 9;
#pragma unroll 1
      for (int s = 5; s < n; s++) fill(wid, t2.ks0, s);
      if (b == 255 && lw == 0) {
#pragma unroll 1
        for (int r = 0; r < 4; r++) {
#pragma unroll 1
          for (int it = 0; it < SPIN_CAP; it++) {
            u64 v = ld_flag(ws, WS_G + (r * 64 + lane) * 128);
            if (__all((unsigned)(v >> 32) == t0)) break;
            __builtin_amdgcn_s_sleep(1);
          }
        }
        if (lane == 0) st_flag(ws, WS_C, tag);
      }
      STAMP(17);
      VMCNT(0);
      STAMP(18);
      BAR();
      STAMP(19);
      if (lw == 3) {
#pragma unroll 1
        for (int s = 7; s < 9; s++) fill(wid, t2.ks0, s);
      }
    }
    phase2(wr, rs, ws, x, b2, out, t2, wid, lane, tag ST_PASS);
    ST_END(ws, 0);
    return;
  }

  const int wm = wid & 1, wn = wid >> 1;
  if (wid == 0) {
    u32x4 xs[2][5];
    const u64 ep = ld_flag(ws, WS_C);
#pragma unroll
    for (int r = 0; r < 2; r++)
#pragma unroll
      for (int c = 0; c < 5; c++)
        xs[r][c] = *(const u32x4*)((const char*)x + (size_t)(2 * (8 * (b >> 1) + 4 * r + g) + p) * 1280 + c * 256 + l15 * 16);
    const unsigned t0 = (unsigned)ep + 1u, t1 = (unsigned)(ep >> 32) + 0x9E3779B1u;
    float st[2][2];
#pragma unroll
    for (int r = 0; r < 2; r++) {
      float a = 0.f, q = 0.f;
#pragma unroll
      for (int c = 0; c < 5; c++)
#pragma unroll
        for (int e = 0; e < 4; e++) { a = dot2(xs[r][c][e], 0x3f803f80u, a); q = dot2(xs[r][c][e], xs[r][c][e], q); }
      a = sum16(a); q = sum16(q);
      float mean = a * (1.f / 640.f);
      float var = fmaxf(q * (1.f / 640.f) - mean * mean, 0.f);
      float rstd = __builtin_amdgcn_rsqf(var + 1e-5f);
      st[r][0] = rstd; st[r][1] = rstd * mean;
    }
    st16(rs, l15 < 2 ? WS_G + (p * 1024 + 8 * (b >> 1) + 4 * l15 + g) * 16 : WS_END,
         u32x4{__float_as_uint(l15 ? st[1][0] : st[0][0]), t0, __float_as_uint(l15 ? st[1][1] : st[0][1]), t1});
    *(LDS u32x2*)OFF_TAG = u32x2{t0, t1};
  }
  unsigned wb[8];
  {
    const int c = lane & 3, a = 80 * wn;
    unsigned cq[8] = {0u, 40u, 176u, 216u, 0u, 336u, 376u, 0u};
    int q = l15 >> 2, f = q | ((g & 1) << 2);
#pragma unroll
    for (int nt = 0; nt < 8; nt++) {
      unsigned cell = nt == 4 ? (c >> 1) * 176 + a + 32 + (c & 1) * 40 : nt == 7 ? 368 + a + (c & 1) * 40 : cq[nt] + a + 8 * c;
      cell = isq ? cell : 256 * wn + 32 * nt + 8 * c;
      wb[nt] = 16384 + wm * 16384 + (8 * g + q) * 512 + (cell ^ (f << 5));
    }
  }
  const unsigned xrow = (64 * wm + l15) * 128;
  const unsigned xb0 = xrow + (((4 * wm + g) ^ (l15 >> 1)) << 4), xb1 = xb0 ^ 64;
  const unsigned wsw = wm ? -16384 : 16384;
  const unsigned csw = OFF_CS + wid * 512 + g * 16;
  f32x4 cs[8];
  f32x4 acc[8][4];
#pragma unroll
  for (int nt = 0; nt < 8; nt++) {
    cs[nt] = f32x4{0.f, 0.f, 0.f, 0.f};
#pragma unroll
    for (int tb = 0; tb < 4; tb++) acc[nt][tb] = f32x4{0.f, 0.f, 0.f, 0.f};
  }
  unsigned fo;
  u32x4 wr[5][5];
  if (!isq) {
    mainloop<false>(acc, cs, wb, xb0, xb1, wsw, csw, l15 == 0 ST_PASS);
    STAMP(15);
    float rs_[4], rm[4];
#pragma unroll
    for (int tb = 0; tb < 4; tb++) {
      rs_[tb] = *(LDS float*)(OFF_ROW + (64 * wm + 16 * tb + l15) * 4);
      rm[tb] = -*(LDS float*)(OFF_ROW + 512 + (64 * wm + 16 * tb + l15) * 4);
    }
    const int rb = 2 * rt + wm;
    const int ks0 = 20 + (colb >> 5) + 4 * wn;
    const int skip = (colb == 1472 && wn == 0) ? 2 : 0;
    const unsigned dst = WS_A + ((p * 16 + rb) * NKS + ks0) * 4096;
#pragma unroll
    for (int pr = 0; pr < 4; pr++) {
      SB();
      ldW(wr[pr], rs, lo, wcol + kstep(t2, t2.ks0, pr) * 5120);
      SB();
      f32x4 c0 = cs[2 * pr], c1 = cs[2 * pr + 1];
      f32x4 b0 = ldsreadf4(OFF_EX + (128 * wn + 32 * pr + 4 * g) * 4), b1v = ldsreadf4(OFF_EX + (128 * wn + 32 * pr + 16 + 4 * g) * 4);
      const unsigned d = (pr >= skip ? dst + pr * 4096 : WS_END) + lo;
#pragma unroll
      for (int tb = 0; tb < 4; tb++) {
        f32x2 v[4], a[4];
        v[0] = fma2(bc2(rs_[tb]), lo2(acc[2 * pr][tb]), fma2(bc2(rm[tb]), lo2(c0), lo2(b0)));
        v[1] = fma2(bc2(rs_[tb]), hi2(acc[2 * pr][tb]), fma2(bc2(rm[tb]), hi2(c0), hi2(b0)));
        v[2] = fma2(bc2(rs_[tb]), lo2(acc[2 * pr + 1][tb]), fma2(bc2(rm[tb]), lo2(c1), lo2(b1v)));
        v[3] = fma2(bc2(rs_[tb]), hi2(acc[2 * pr + 1][tb]), fma2(bc2(rm[tb]), hi2(c1), hi2(b1v)));
#pragma unroll
        for (int r = 0; r < 4; r++) a[r] = v[r] * fma2(v[r] * v[r], bc2(-0.10294324f), bc2(-2.30220820f));
#pragma unroll
        for (int r = 0; r < 4; r++) a[r] = f32x2{__builtin_amdgcn_exp2f(a[r][0]), __builtin_amdgcn_exp2f(a[r][1])} + bc2(1.f);
#pragma unroll
        for (int r = 0; r < 4; r++) v[r] *= f32x2{__builtin_amdgcn_rcpf(a[r][0]), __builtin_amdgcn_rcpf(a[r][1])};
        st16(rs, d + tb * 1024, u32x4{pk2(v[0]), pk2(v[1]), pk2(v[2]), pk2(v[3])});
      }
    }
    fo = WS_FH + ((p * 16 + rb) * 16 + 2 * ct + wn) * 8;
  } else {
    mainloop<true>(acc, cs, wb, xb0, xb1, wsw, csw, l15 == 0 ST_PASS);
    STAMP(15);
    const float qsc = 0.22360679775f * 1.44269504089f;
    u32x2 T[8][5];
    u32x2 XK[5];
    const float xs = g < 2 ? qsc : 1.f;
#pragma unroll
    for (int tq = 0; tq < 4; tq++) {
      const int tb = 3 - tq;
      float rs1 = *(LDS float*)(OFF_ROW + (64 * wm + 16 * tb + l15) * 4);
      float rm = -*(LDS float*)(OFF_ROW + 512 + (64 * wm + 16 * tb + l15) * 4);
      f32x4 rs4 = ldsreadf4(OFF_ROW + (64 * wm + 16 * tb + 4 * g) * 4);
      f32x4 rm4 = -ldsreadf4(OFF_ROW + 512 + (64 * wm + 16 * tb + 4 * g) * 4);
#pragma unroll
      for (int nt = 0; nt < 5; nt++) {
        float a = nt < 2 ? rs1 * qsc : nt < 4 ? rs1 : rs1 * xs;
        float m = nt < 2 ? rm * qsc : nt < 4 ? rm : rm * xs;
        T[nt][tb + 1] = u32x2{pk2(fma2(bc2(a), lo2(acc[nt][tb]), bc2(m) * lo2(cs[nt]))), pk2(fma2(bc2(a), hi2(acc[nt][tb]), bc2(m) * hi2(cs[nt])))};
      }
#pragma unroll
      for (int nt = 5; nt < 8; nt++)
        T[nt][tb + 1] = u32x2{pk2(fma2(lo2(rs4), lo2(acc[nt][tb]), lo2(rm4) * bc2(cs[nt][0]))), pk2(fma2(hi2(rs4), hi2(acc[nt][tb]), hi2(rm4) * bc2(cs[nt][0])))};
#pragma unroll
      for (int e = 0; e < 2; e++) {
        auto r = __builtin_amdgcn_permlane32_swap(T[4][tb + 1][e], T[4][tb + 1][e], false, false);
        XK[tb + 1][e] = r[1];
      }
      if (tb == 3 && wm == 0) {
        unsigned ex = OFF_EX + wn * 3072 + lo;
        *(LDS u32x4*)(ex) = u32x4{T[2][4][0], T[2][4][1], T[3][4][0], T[3][4][1]};
        *(LDS u32x4*)(ex + 1024) = u32x4{XK[4][0], XK[4][1], T[5][4][0], T[5][4][1]};
        *(LDS u32x4*)(ex + 2048) = u32x4{T[6][4][0], T[6][4][1], T[7][4][0], T[7][4][1]};
        asm volatile("s_waitcnt lgkmcnt(0)" ::: "memory");
        *(volatile LDS unsigned*)(OFF_TAG + 8 + wn * 4) = 1u;
      }
    }
    while (*(volatile LDS unsigned*)(OFF_TAG + 8 + wn * 4) == 0u) __builtin_amdgcn_s_sleep(1);
    asm volatile("" ::: "memory");
    {
      unsigned ex = OFF_EX + wn * 3072 + lo;
      u32x4 a = ldsread128(ex), bq = ldsread128(ex + 1024), c = ldsread128(ex + 2048);
      T[2][0] = u32x2{a[0], a[1]}; T[3][0] = u32x2{a[2], a[3]};
      XK[0] = u32x2{bq[0], bq[1]}; T[5][0] = u32x2{bq[2], bq[3]};
      T[6][0] = u32x2{c[0], c[1]}; T[7][0] = u32x2{c[2], c[3]};
    }
    STAMP(16);
    SB();
#pragma unroll
    for (int s = 0; s < 4; s++) ldW(wr[s], rs, lo, wcol + kstep(t2, t2.ks0, s) * 5120);
    SB();
    f32x4 mP, mC, mP0; unsigned sel[2];
    {
      const float NEG = -1e30f;
#pragma unroll
      for (int r = 0; r < 4; r++) {
        int jj = 4 * g + r;
        mC[r] = (jj <= l15 && jj >= l15 - 7) ? 0.f : NEG;
        mP[r] = (jj >= l15 + 9) ? 0.f : NEG;
        mP0[r] = wm == 0 ? NEG : mP[r];
      }
#pragma unroll
      for (int e = 0; e < 2; e++) sel[e] = ((4 * g + 2 * e > l15) ? 0xffffu : 0u) | ((4 * g + 2 * e + 1 > l15) ? 0xffff0000u : 0u);
    }
    const unsigned gm0 = g == 0 ? 0xffffffffu : 0u, gm1 = g == 1 ? 0xffffffffu : 0u;
    const u32x4 ones = {0x3f803f80u, 0x3f803f80u, 0x3f803f80u, 0x3f803f80u};
    const f32x4 z4 = {0.f, 0.f, 0.f, 0.f};
    const int hp = 2 * hg + wn;
#pragma unroll
    for (int half = 0; half < 2; half++) {
      f32x4 sv[4]; float mx[4]; u32x4 P[4];
#pragma unroll
      for (int un = 0; un < 4; un++) {
        const int tb = 3 - 2 * half - (un >> 1), h = un & 1;
        unsigned gm = h ? gm1 : gm0;
        u32x4 Q = {T[h][tb + 1][0], T[h][tb + 1][1], T[4][tb + 1][0] & gm, T[4][tb + 1][1] & gm};
        u32x4 Kp = {T[2 + h][tb][0], T[2 + h][tb][1], XK[tb][0], XK[tb][1]};
        u32x4 Kc = {T[2 + h][tb + 1][0], T[2 + h][tb + 1][1], XK[tb + 1][0], XK[tb + 1][1]};
        f32x4 sp = mfma(Kp, Q, tb == 0 ? mP0 : mP), sc = mfma(Kc, Q, mC);
        sv[un] = __builtin_elementwise_max(sp, sc);
      }
#pragma unroll
      for (int un = 0; un < 4; un++) mx[un] = xmax(fmaxf(fmaxf(sv[un][0], sv[un][1]), fmaxf(sv[un][2], sv[un][3])));
#pragma unroll
      for (int un = 0; un < 4; un++) {
        unsigned p0 = pk(__builtin_amdgcn_exp2f(sv[un][0] - mx[un]), __builtin_amdgcn_exp2f(sv[un][1] - mx[un]));
        unsigned p1 = pk(__builtin_amdgcn_exp2f(sv[un][2] - mx[un]), __builtin_amdgcn_exp2f(sv[un][3] - mx[un]));
        P[un] = u32x4{p0 & sel[0], p1 & sel[1], p0 & ~sel[0], p1 & ~sel[1]};
      }
      f32x4 sum[4], oa[4], ob[4];
#pragma unroll
      for (int un = 0; un < 4; un++) {
        const int tb = 3 - 2 * half - (un >> 1), h = un & 1;
        u32x4 V = {T[5 + h][tb][0], T[5 + h][tb][1], T[5 + h][tb + 1][0], T[5 + h][tb + 1][1]};
        u32x4 V2 = {T[7][tb][0], T[7][tb][1], T[7][tb + 1][0], T[7][tb + 1][1]};
        sum[un] = mfma(ones, P[un], z4); oa[un] = mfma(V, P[un], z4); ob[un] = mfma(V2, P[un], z4);
      }
#pragma unroll
      for (int k = 0; k < 2; k++) {
        const int tb = 3 - 2 * half - k;
        u32x2 o[2], o2[2];
#pragma unroll
        for (int h = 0; h < 2; h++) {
          f32x2 ri = bc2(__builtin_amdgcn_rcpf(sum[2 * k + h][0]));
          o[h] = u32x2{pk2(lo2(oa[2 * k + h]) * ri), pk2(hi2(oa[2 * k + h]) * ri)};
          o2[h] = u32x2{pk2(lo2(ob[2 * k + h]) * ri), pk2(hi2(ob[2 * k + h]) * ri)};
        }
        const int B = 7 * rt + 4 * wm + tb;
        unsigned dst = WS_A + ((p * 16 + (B >> 2)) * NKS) * 4096 + (B & 3) * 1024;
        if (tb == 0) dst = (wm == 0 && rt != 0) ? WS_END : dst;
        st16(rs, dst + hp * 4096 + lo, u32x4{o[0][0], o[0][1], o[1][0], o[1][1]});
        auto r0 = __builtin_amdgcn_permlane16_swap(o2[1][0], o2[1][0], false, false);
        auto r1 = __builtin_amdgcn_permlane16_swap(o2[1][1], o2[1][1], false, false);
        st16(rs, g == 0 ? dst + (16 + (hp >> 2)) * 4096 + (hp & 3) * 256 + lo : WS_END, u32x4{o2[0][0], o2[0][1], r0[1], r1[1]});
      }
    }
    fo = WS_FQ + ((p * 18 + 2 * rt + wm) * 16 + hp) * 8;
  }
  const u32x2 tg = *(LDS u32x2*)OFF_TAG;
  const u64 tag = ((u64)__builtin_amdgcn_readfirstlane(tg[1]) << 32) | (unsigned)__builtin_amdgcn_readfirstlane(tg[0]);
  STAMP(17);
  VMCNT(0);
  STAMP(18);
  if (lane == 0) st_flag(ws, fo, tag);
  SB();
  ldW(wr[4], rs, lo, wcol + kstep(t2, t2.ks0, 4) * 5120);
  SB();
  BAR();
  STAMP(19);
  phase2(wr, rs, ws, x, b2, out, t2, wid, lane, tag ST_PASS);
  ST_END(ws, 0);
}

__global__ void tiny(char* ws) { if (threadIdx.x == 9999) ws[WS_END] = 1; }
extern "C" void kernel_launch(void* const* d_in, const int* in_sizes, int n_in, void* d_out, int out_size, void* d_ws, size_t ws_size,
                              hipStream_t stream) {
  const u16* x = (const u16*)d_in[0]; const u16* wqkv = (const u16*)d_in[1]; const u16* wo = (const u16*)d_in[2];
  const u16* w1 = (const u16*)d_in[4]; const u16* b1 = (const u16*)d_in[5]; const u16* w2 = (const u16*)d_in[6]; const u16* b2 = (const u16*)d_in[7];
  (void)hipFuncSetAttribute((const void*)fused, hipFuncAttributeMaxDynamicSharedMemorySize, LDS_BYTES);
  fused<<<256, 512, LDS_BYTES, stream>>>(x, wqkv, w1, b1, wo, w2, b2, (char*)d_ws, (u16*)d_out);
}
```

```cpp
#include <hip/hip_runtime.h>
#include <hip/hip_bf16.h>
#pragma clang diagnostic ignored "-Wint-to-pointer-cast"
typedef unsigned short u16;
typedef unsigned long long u64;
typedef short s16x4 __attribute__((ext_vector_type(4)));
typedef __bf16 bf16x2 __attribute__((ext_vector_type(2)));
typedef __bf16 bf16x8 __attribute__((ext_vector_type(8)));
typedef float f32x4 __attribute__((ext_vector_type(4)));
typedef float f32x2 __attribute__((ext_vector_type(2)));
typedef unsigned u32x2 __attribute__((ext_vector_type(2)));
typedef unsigned u32x4 __attribute__((ext_vector_type(4)));
typedef __attribute__((address_space(1))) u64 gu64;
#define LDS __attribute__((address_space(3)))
#define DEV __device__ __forceinline__
#define RLX_AGENT __ATOMIC_RELAXED, __HIP_MEMORY_SCOPE_AGENT

constexpr int D_ = 640, F_ = 1728, NQKV = 1920;
constexpr int STG = 49152;
constexpr int NKS = 74;
constexpr int OFF_ROW = 158720;
constexpr int OFF_CS = OFF_ROW + 1024;
constexpr int OFF_EX = OFF_CS + 1024;
constexpr int OFF_EX0 = 138240;
constexpr int OFF_TAG = OFF_EX + 2816;
constexpr int LDS_BYTES = 163840;
constexpr unsigned WS_A = 0;
constexpr unsigned WS_W = 32 * NKS * 4096;
constexpr unsigned WS_C = WS_W + 8 * NKS * 5120;
#define FSTR 8
constexpr unsigned WS_FW = WS_C + 2048, WS_G = WS_C + 16384, WS_CG = WS_G + 32768;
constexpr unsigned WS_FH = WS_CG + 131072, WS_FQ = WS_FH + 1024 * FSTR;
constexpr unsigned WS_END = WS_FQ + 576 * FSTR;
constexpr int SPIN_CAP = 1 << 20;
#define CODE_SPAN 55000
#define WAUX 16
#define WSC " sc1"
#define POLL_SLEEP 6
#define AAUX 16
#define ABASE abase
#define YB1 9
#define YB2 11
#define LATE 4
#define EPUB 2
#define RING 4
#define NEX 4
#define AD 3
#define SPRIO 2
#define WI8 1
#define PDELAY 0
#define QDL 8
#define WDL 8
#define QTRIG 2
#define WTRIG 3
#define MAP2 0
#define QPACE 12
#define WPACE 6

DEV void dma16(unsigned lds, unsigned voff, const void* base) {
  asm volatile("s_mov_b32 m0, %0\n\ts_nop 0\n\tglobal_load_lds_dwordx4 %1, %2" ::"s"(lds), "v"(voff), "s"(base) : "memory");
}
template <unsigned LOFF, unsigned GOFF>
DEV void dmab(unsigned ldsb, unsigned voff, u32x4 srd) {
  unsigned t;
  asm volatile("s_add_u32 m0, %1, %4\n\ts_mov_b32 %0, %5\n\tbuffer_load_dwordx4 %2, %3, %0 offen lds"
               : "=&s"(t) : "s"(ldsb), "v"(voff), "s"(srd), "i"(LOFF), "i"(GOFF) : "memory", "scc");
}
DEV void dmas(unsigned lds, unsigned voff, u32x4 srd, unsigned soff) {
  asm volatile("s_mov_b32 m0, %0\n\ts_nop 0\n\tbuffer_load_dwordx4 %1, %2, %3 offen lds" ::"s"(lds), "v"(voff), "s"(srd), "s"(soff) : "memory");
}
#define ZOOB 0x80000000u
DEV u32x4 mksrd(const void* p) { u64 a = (u64)p; return u32x4{(unsigned)a, (unsigned)(a >> 32) & 0xffffu, 0x7fffffffu, 0x00020000u}; }
template <int A, int E, int LDW> struct Run {
  static DEV void go(unsigned lb, unsigned xv0, unsigned xv, unsigned wv0, unsigned wv1, u32x4 xs, u32x4 wsr) {
    if constexpr (A < E) {
      constexpr int s = A / 12, m = (A % 12) / 3, e = A % 3;
      if constexpr (e == 0) dmab<(s % 3) * STG + m * 4096, m * 64 * 1280 + s * 128>(lb, m == 0 ? xv0 : xv, xs);
      else dmab<(s % 3) * STG + 16384 + (2 * m + e - 1) * 4096, (s * 64 + 8 * (2 * m + e - 1)) * LDW>(lb, e == 1 ? wv0 : wv1, wsr);
      Run<A + 1, E, LDW>::go(lb, xv0, xv, wv0, wv1, xs, wsr);
    }
  }
};
DEV void dma16c(unsigned lds, unsigned voff, const void* base) {
  asm volatile("s_mov_b32 m0, %0\n\ts_nop 0\n\tglobal_load_lds_dwordx4 %1, %2" WSC ::"s"(lds), "v"(voff), "s"(base) : "memory");
}
DEV f32x4 mfma(u32x4 a, u32x4 b, f32x4 c) {
  return __builtin_amdgcn_mfma_f32_16x16x32_bf16(__builtin_bit_cast(bf16x8, a), __builtin_bit_cast(bf16x8, b), c, 0, 0, 0);
}
DEV u32x2 trread(unsigned addr) {
  return __builtin_bit_cast(u32x2, __builtin_amdgcn_ds_read_tr16_b64_v4i16((LDS s16x4*)addr));
}
DEV u32x4 ldsread128(unsigned addr) { return *(LDS u32x4*)addr; }
DEV f32x4 ldsreadf4(unsigned addr) { return *(LDS f32x4*)addr; }
DEV float ldsreadf(unsigned addr) { return *(LDS float*)addr; }
DEV unsigned pk(float a, float b) { f32x2 v = {a, b}; return __builtin_bit_cast(unsigned, __builtin_convertvector(v, bf16x2)); }
#define SB() __builtin_amdgcn_sched_barrier(0)
#define BAR() asm volatile("s_waitcnt lgkmcnt(0)\n\ts_barrier" ::: "memory")
#define STR_(x) #x
#define VMCNT(n) asm volatile("s_waitcnt vmcnt(" STR_(n) ")" ::: "memory")
#define WAITV(n) asm volatile("s_waitcnt vmcnt(%0)" ::"i"(n) : "memory")
#define ST_INIT()
#define STAMP(i)
#define ST_END(base, kern)
#define ST_ARG
#define ST_PASS
DEV float fmax2(float a, float b) { return __builtin_elementwise_maximum(a, b); }
DEV float asf(unsigned u) { return __uint_as_float(u); }
DEV unsigned asu(float f) { return __float_as_uint(f); }
DEV void xmax4(float (&m)[4]) {
  auto a = __builtin_amdgcn_permlane32_swap(asu(m[0]), asu(m[1]), false, false);
  auto b = __builtin_amdgcn_permlane32_swap(asu(m[2]), asu(m[3]), false, false);
  float u = fmax2(asf(a[0]), asf(a[1])), v = fmax2(asf(b[0]), asf(b[1]));
  auto c = __builtin_amdgcn_permlane16_swap(asu(u), asu(v), false, false);
  float w = fmax2(asf(c[0]), asf(c[1]));
  auto d = __builtin_amdgcn_permlane16_swap(asu(w), asu(w), false, false);
  auto e = __builtin_amdgcn_permlane32_swap(d[0], d[0], false, false);
  auto f = __builtin_amdgcn_permlane32_swap(d[1], d[1], false, false);
  m[0] = asf(e[0]); m[1] = asf(e[1]); m[2] = asf(f[0]); m[3] = asf(f[1]);
}
DEV void xmax8(float (&m)[2][4]) {
  unsigned u[2], v[2], w[2];
#pragma unroll
  for (int k = 0; k < 2; k++) {
    auto a = __builtin_amdgcn_permlane32_swap(asu(m[k][0]), asu(m[k][1]), false, false);
    auto b = __builtin_amdgcn_permlane32_swap(asu(m[k][2]), asu(m[k][3]), false, false);
    m[k][0] = asf(a[0]); m[k][1] = asf(a[1]); m[k][2] = asf(b[0]); m[k][3] = asf(b[1]);
  }
  SB();
#pragma unroll
  for (int k = 0; k < 2; k++) { u[k] = asu(fmax2(m[k][0], m[k][1])); v[k] = asu(fmax2(m[k][2], m[k][3])); }
  SB();
#pragma unroll
  for (int k = 0; k < 2; k++) { auto c = __builtin_amdgcn_permlane16_swap(u[k], v[k], false, false); w[k] = asu(fmax2(asf(c[0]), asf(c[1]))); }
  SB();
#pragma unroll
  for (int k = 0; k < 2; k++) { auto d = __builtin_amdgcn_permlane16_swap(w[k], w[k], false, false); u[k] = d[0]; v[k] = d[1]; }
  SB();
#pragma unroll
  for (int k = 0; k < 2; k++) {
    auto e = __builtin_amdgcn_permlane32_swap(u[k], u[k], false, false);
    auto f = __builtin_amdgcn_permlane32_swap(v[k], v[k], false, false);
    m[k][0] = asf(e[0]); m[k][1] = asf(e[1]); m[k][2] = asf(f[0]); m[k][3] = asf(f[1]);
  }
}
DEV float add1(float a, float b) { float r; asm("v_add_f32 %0, %1, %2" : "=v"(r) : "v"(a), "v"(b)); return r; }
DEV float fmac1(float a, float c) { asm("v_fmac_f32 %0, %1, %1" : "+v"(c) : "v"(a)); return c; }
template <int N> DEV float ror16(float v) {
  return __uint_as_float(__builtin_amdgcn_update_dpp(0u, __float_as_uint(v), 0x120 + N, 0xf, 0xf, false));
}
DEV float dot2(unsigned a, unsigned b, float c) {
  return __builtin_amdgcn_fdot2_f32_bf16(__builtin_bit_cast(bf16x2, a), __builtin_bit_cast(bf16x2, b), c, false);
}
DEV float blo(unsigned u) { return __uint_as_float(u << 16); }
DEV float bhi(unsigned u) { return __uint_as_float(u & 0xffff0000u); }
#define PMODE 0
DEV u64 ld_flag(const char* ws, unsigned off) {
  return __hip_atomic_load((gu64*)(ws + off), RLX_AGENT);
}
DEV void st_flag(char* ws, unsigned off, u64 v) { __hip_atomic_store((gu64*)(ws + off), v, RLX_AGENT); }
DEV void wait_lds(unsigned word) {
  unsigned v;
  asm volatile(".Lw%=:\n\tds_read_b32 %0, %1\n\ts_waitcnt lgkmcnt(0)\n\tv_cmp_eq_u32 vcc, 0, %0\n\ts_cbranch_vccz .Ld%=\n\t"
               "s_sleep 1\n\ts_branch .Lw%=\n.Ld%=:" : "=&v"(v) : "v"(word) : "memory", "vcc");
}
DEV void raise(unsigned word) { asm volatile("" ::: "memory"); *(volatile LDS unsigned*)word = 1u; asm volatile("" ::: "memory"); }
constexpr int LW_HALO = OFF_TAG + 8, LW_Q = OFF_TAG + 16, LW_EARLY = OFF_TAG + 20, LW_LATE = OFF_TAG + 24;
constexpr int LW_FILL = OFF_TAG + 32, LW_FREE1 = OFF_TAG + 48, LW_EX = OFF_TAG + 64, LW_FREE2 = OFF_TAG + 80, LW_SP = OFF_TAG + 96, LW_DONE = OFF_TAG + 112;
DEV void poll_flags(const char* ws, unsigned off, bool need, u64 tag, unsigned word) {
#pragma unroll 1
  for (int it = 0; it < SPIN_CAP; it++) {
    u64 v = ld_flag(ws, off);
    if (__builtin_amdgcn_ballot_w64(need && v != tag) == 0) break;
    __builtin_amdgcn_s_sleep(POLL_SLEEP);
  }
  asm volatile("" ::: "memory");
  *(volatile LDS unsigned*)word = 1u;
}

DEV f32x2 lo2(f32x4 v) { return __builtin_shufflevector(v, v, 0, 1); }
DEV f32x2 hi2(f32x4 v) { return __builtin_shufflevector(v, v, 2, 3); }
DEV f32x2 fma2(f32x2 a, f32x2 b, f32x2 c) { return __builtin_elementwise_fma(a, b, c); }
DEV f32x2 bc2(float a) { return f32x2{a, a}; }
DEV unsigned pk2(f32x2 v) { return __builtin_bit_cast(unsigned, __builtin_convertvector(v, bf16x2)); }
DEV u32x4 cat(u32x2 a, u32x2 b) { return u32x4{a[0], a[1], b[0], b[1]}; }
#define PIN(x) asm volatile("" : "+v"(x))
template <bool QKV>
DEV void mainloop(f32x4 (&acc)[8][4], const unsigned (&wb)[8], unsigned xb0 ST_ARG) {
  u32x2 wl[2][4], wh[2][4];
  u32x4 xf[8];
  unsigned wa[8], xa0 = xb0, xa1 = xb0 ^ 64;
#pragma unroll
  for (int nt = 0; nt < 8; nt++) {
    wa[nt] = wb[nt];
#pragma unroll
    for (int tb = 0; tb < 4; tb++) PIN(acc[nt][tb]);
  }
  auto rw = [&](int P, int i) {
    const int f = i >> 1; const unsigned a = wa[2 * P + (f >> 1)] + (f & 1) * 16384;
    if (i & 1) wh[P & 1][f] = trread(a + 2048); else wl[P & 1][f] = trread(a);
  };
  auto rx = [&](int i) { xf[i] = ldsread128(((i & 1) ? xa1 : xa0) + (i >> 1) * 2048); };
  __builtin_amdgcn_s_waitcnt(0xC07F);
  STAMP(2);
  BAR();
  STAMP(3);
  SB();
  rw(0, 0); rw(0, 1); rx(0); rw(0, 2); rw(0, 3); rx(1);
#pragma unroll
  for (int i = 4; i < 8; i++) rw(0, i);
#pragma unroll
  for (int i = 2; i < 6; i++) rx(i);
  int ph = 0;
#pragma unroll 1
  for (int s = 0; s < 10; s++) {
    int dl;
#pragma unroll
    for (int P = 0; P < 4; P++) {
#pragma unroll
      for (int i = 0; i < 16; i++) {
        const int tb = i >> 2, nt = 2 * P + ((i >> 1) & 1), f = i & 3, k = i & 1;
        const u32x4 w = cat(wl[P & 1][f], wh[P & 1][f]);
        SB();
        if (QKV && nt >= 5) acc[nt][tb] = mfma(xf[2 * tb + k], w, acc[nt][tb]);
        else acc[nt][tb] = mfma(w, xf[2 * tb + k], acc[nt][tb]);
        SB();
        auto adv = [&](unsigned& a) { a += dl; PIN(a); };
        if (P == 0) {
          if (i < 2) rx(6 + i);
          else if (i < 10) rw(1, i - 2);
          else if (i == 10) { ph = ph == 2 ? 0 : ph + 1; dl = ph == 0 ? -2 * STG : STG; asm volatile("" : "+s"(dl)); }
          else if (i == 12) adv(xa0);
          else if (i == 13) adv(xa1);
        } else if (P < 3) {
          if (i < 8) rw(P + 1, i);
          else if (i < 12) adv(wa[4 * (P - 1) + i - 8]);
        } else {
          if (i < 3) rw(0, i);
          else if ((i & 3) == 3 || (i & 3) == 0) { if (i < 15) rx(2 * ((i - 3) >> 2) + ((i + 1) & 1)); }
          else if (i < 14) rw(0, i - 2 - 2 * ((i - 5) >> 2));
        }
      }
      if (P == 2) {
        SB();
        BAR();
      }
    }
  }
  SB();
}

struct P2 { int p, rb, cb, rot; };
DEV int ystep(const P2& t, int i, int s, int xs) {
  int k = (s > xs ? s - 1 : s) + ((t.rot * 13) >> 3); k = k >= 13 ? k - 13 : k;
  return 20 + 13 * i + (i < 2 ? i : 2) + (s != xs ? k : 13);
}
DEV int sstep(const P2& t, int i, int s) { int k = s + ((t.rot * 5) >> 3); k = k >= 5 ? k - 5 : k; return 5 * i + k; }

template <typename R>
DEV void ldW(u32x4 (&w)[5], R rs, int lo, unsigned soff) {
#pragma unroll
  for (int i = 0; i < 5; i++) w[i] = __builtin_amdgcn_raw_buffer_load_b128(rs, lo + (i & 3) * 1024, soff + (i >> 2) * 4096, WAUX);
}
template <typename R>
DEV void st_flag1(R rs, int lane, unsigned off, u64 v) {
  __builtin_amdgcn_raw_buffer_store_b64(u32x2{(unsigned)v, (unsigned)(v >> 32)}, rs, lane == 0 ? off : WS_END, 0, 16);
}
template <typename R>
DEV void st16(R rs, unsigned off, u32x4 v) { __builtin_amdgcn_raw_buffer_store_b128(v, rs, off, 0, 16); }

DEV void put_partial(const f32x4 (&acc)[5][4], int region, int lane) {
  const unsigned wa = region * 20480 + (lane & 15) * 320 + (((lane >> 4) ^ ((lane >> 1) & 3)) << 4);
#pragma unroll
  for (int tb = 0; tb < 4; tb++)
#pragma unroll
    for (int i = 0; i < 5; i++) *(LDS f32x4*)(wa + tb * 5120 + i * 64) = acc[i][tb];
}
DEV void poll2(const char* ws, unsigned off, u64 tag) {
#pragma unroll 1
  for (int it = 0; it < SPIN_CAP; it++) {
    u64 miss = __builtin_amdgcn_ballot_w64(ld_flag(ws, off) != tag);
    if (it == SPIN_CAP - 1) miss = 0;
    asm volatile("" ::: "memory");
    if ((unsigned)miss == 0u) raise(LW_Q);
    if ((miss >> 32) == 0u) raise(LW_LATE);
    if (miss == 0) break;
    __builtin_amdgcn_s_sleep(POLL_SLEEP);
  }
}
template <int NPRE, int TRIG, typename R>
DEV void phase2_y(R rs, P2 t, int wid, int lane, unsigned fo, u64 tag ST_ARG) {
  const int lo = lane * 16;
  constexpr int XS = NPRE + 7;
  const unsigned abase = WS_A + (t.p * 16 + t.rb) * NKS * 4096, wcol = WS_W + t.cb * NKS * 5120;
  u32x4 af[14][4], wr[5][5];
  auto ldA1 = [&](int s, int i) {
    af[s][i] = __builtin_amdgcn_raw_buffer_load_b128(rs, lo + i * 1024, (s != XS || wid < 2) ? abase + ystep(t, wid, s, XS) * 4096 : WS_END, 16);
  };
  auto getW1 = [&](int s, int i) {
    const int k = s - NPRE;
    if (k >= 0 && k < 8) wr[s % 5][i] = ldsread128((wid + (k >> 2) * 4) * 20480 + lo + ((k == 7 && wid >= 2) ? 2 : (k & 3)) * 5120 + i * 1024);
    else wr[s % 5][i] = __builtin_amdgcn_raw_buffer_load_b128(rs, lo + (i & 3) * 1024, wcol + ystep(t, wid, s, XS) * 5120 + (i >> 2) * 4096, 16);
  };
  VMCNT(0);
  STAMP(18);
  st_flag1(rs, lane, fo, tag);
  SB();
#pragma unroll
  for (int s = 0; s < NPRE; s++) {
    if (s == TRIG) raise(LW_EARLY);
#pragma unroll
    for (int i = 0; i < 5; i++) getW1(s, i);
    SB();
  }
  if (TRIG >= NPRE) raise(LW_EARLY);
  STAMP(19);
  wait_lds((lane & 1) ? LW_LATE : LW_FILL + wid * 4);
  STAMP(21);
#pragma unroll
  for (int s = 0; s < 3; s++) {
#pragma unroll
    for (int i = 0; i < 4; i++) ldA1(s, i);
    SB();
  }
#pragma unroll
  for (int s = NPRE; s < 5; s++)
#pragma unroll
    for (int i = 0; i < 5; i++) getW1(s, i);
  SB();
  STAMP(20);
  const unsigned pa = (wid + 4) * 20480 + (lane & 15) * 320 + (((lane >> 4) ^ ((lane >> 1) & 3)) << 4);
  f32x4 acc[5][4];
#pragma unroll
  for (int s = 0; s < 14; s++) {
#pragma unroll
    for (int tb = 0; tb < 4; tb++) {
#pragma unroll
      for (int i = 0; i < 5; i++) {
        acc[i][tb] = mfma(wr[s % 5][i], af[s][tb], s == 0 ? f32x4{0.f, 0.f, 0.f, 0.f} : acc[i][tb]);
        SB();
        if (tb == 0 && s >= 1 && s + 4 < 14) getW1(s + 4, i);
        if (s == 13 && tb >= 1) *(LDS f32x4*)(pa + (tb - 1) * 5120 + i * 64) = acc[i][tb - 1];
        SB();
      }
      if (s + 3 < 14) ldA1(s + 3, tb);
      if (s == (NPRE > 1 ? NPRE - 1 : 0) && tb == 0) raise(LW_FREE2 + wid * 4);
      SB();
    }
  }
  STAMP(22);
  if (wid == 3) wait_lds(LW_DONE + (lane & 3) * 4);
#pragma unroll
  for (int i = 0; i < 5; i++) *(LDS f32x4*)(pa + 3 * 5120 + i * 64) = acc[i][3];
  STAMP(23);
  BAR();
  STAMP(24);
}
template <typename R>
DEV void phase2_s(u32x4 (&wr)[5][5], const u32x2 (&xv)[5], const u32x2 (&bv)[5], const unsigned (&goff)[5], R rs, u16* out, P2 t, int lw, int lane ST_ARG) {
  const unsigned abase = WS_A + (t.p * 16 + t.rb) * NKS * 4096;
  u32x4 af[5][4];
  wait_lds(LW_Q);
  STAMP(20);
  auto lda = [&](int s, int i) { af[s][i] = __builtin_amdgcn_raw_buffer_load_b128(rs, lane * 16 + i * 1024, abase + sstep(t, lw, s) * 4096, 16); };
#pragma unroll
  for (int q = 0; q < 10; q++) { lda(q >> 2, q & 3); SB(); }
  STAMP(21);
  __builtin_amdgcn_s_setprio(SPRIO);
  f32x4 acc[5][4];
#pragma unroll
  for (int s = 0; s < 5; s++) {
#pragma unroll
    for (int tb = 0; tb < 4; tb++) {
#pragma unroll
      for (int i = 0; i < 5; i++) acc[i][tb] = mfma(wr[s][i], af[s][tb], s == 0 ? f32x4{0.f, 0.f, 0.f, 0.f} : acc[i][tb]);
      SB();
      const int q = 10 + 4 * s + tb;
      if (s == 0) lda(q >> 2, q & 3);
      if (s == 1 && tb < 3) { lda((14 + 2 * tb) >> 2, (14 + 2 * tb) & 3); lda((15 + 2 * tb) >> 2, (15 + 2 * tb) & 3); }
      SB();
    }
  }
  STAMP(22);
  __builtin_amdgcn_s_setprio(0);
  STAMP(10);
  wait_lds(LW_FREE2 + lw * 4);
  STAMP(11);
  put_partial(acc, lw, lane);
  raise(LW_SP + lw * 4);
  SB();
  f32x4 sv[5], xf[5], bf[5];
#pragma unroll
  for (int r = 0; r < 5; r++) {
    xf[r] = f32x4{blo(xv[r][0]), bhi(xv[r][0]), blo(xv[r][1]), bhi(xv[r][1])};
    bf[r] = f32x4{blo(bv[r][0]), bhi(bv[r][0]), blo(bv[r][1]), bhi(bv[r][1])};
    PIN(xf[r]); PIN(bf[r]);
  }
  STAMP(12);
  wait_lds(LW_SP + (lane & 3) * 4);
  raise(LW_DONE + lw * 4);
  STAMP(13);
  const unsigned a = (lw * 64 + lane) * 16;
#pragma unroll
  for (int r = 0; r < 5; r++) {
    f32x4 p0 = ldsreadf4(a + r * 4096), p1 = ldsreadf4(a + r * 4096 + 20480), p2 = ldsreadf4(a + r * 4096 + 2 * 20480), p3 = ldsreadf4(a + r * 4096 + 3 * 20480);
#pragma unroll
    for (int e = 0; e < 4; e++) sv[r][e] = add1(add1(p0[e], p1[e]), add1(p2[e], p3[e]));
    PIN(sv[r]);
  }
  STAMP(23);
  BAR();
  STAMP(24);
  f32x4 y[5][4];
#pragma unroll
  for (int r = 0; r < 5; r++)
#pragma unroll
    for (int k = 0; k < 4; k++) y[r][k] = ldsreadf4(a + r * 4096 + (4 + k) * 20480);
  SB();
#pragma unroll
  for (int r = 0; r < 5; r++) {
    const f32x4 yy = ((y[r][0] + y[r][1]) + (y[r][2] + y[r][3])) + bf[r];
    f32x2 e0 = lo2(yy) * bc2(-1.44269504089f), e1 = hi2(yy) * bc2(-1.44269504089f);
    e0 = f32x2{__builtin_amdgcn_exp2f(e0[0]), __builtin_amdgcn_exp2f(e0[1])} + bc2(1.f);
    e1 = f32x2{__builtin_amdgcn_exp2f(e1[0]), __builtin_amdgcn_exp2f(e1[1])} + bc2(1.f);
    const f32x4 sy = sv[r] * yy;
    e0 = fma2(lo2(sy), f32x2{__builtin_amdgcn_rcpf(e0[0]), __builtin_amdgcn_rcpf(e0[1])}, lo2(xf[r]));
    e1 = fma2(hi2(sy), f32x2{__builtin_amdgcn_rcpf(e1[0]), __builtin_amdgcn_rcpf(e1[1])}, hi2(xf[r]));
    *(u32x2*)((char*)out + goff[r]) = u32x2{pk2(e0), pk2(e1)};
  }
  STAMP(25);
}

DEV int qkvoff(int c) {
  int oq = c * 16 + (c > 10 ? 1280 - 176 : 0) + (c > 20 ? 1280 - 160 : 0);
  return (c == 10 || c == 31) ? 0 : oq;
}

#define P2MAP t2.rb = 4 * qd + (j >> 3); t2.cb = j & 7; t2.rot = t2.cb
#define P2SETUP()                                                                     \
  P2 t2; t2.p = p; P2MAP;                                                             \
  const unsigned wcol = WS_W + t2.cb * NKS * 5120;                                    \
  const int lo = lane * 16
#define RPOS 0
#define QPRE 2
#define WPRE 3
#define SEARLY 1
#define QD0 6
#define QD1 12
#define QD8 5
#define WD0 6
#define WD1 6
#define NEARLY 6
template <bool QKV>
DEV void mfma_role(const u32x2 (&xs)[5], const u32x4 (&cw)[5], u64 ep, char* ws, int b, int wid, int lane ST_ARG) {
  const int xcd = b & 7, j = b >> 3, p = xcd & 1, qd = xcd >> 1;
  const int u1 = 14 * qd + (j - 18);
  const int hg = 2 * qd + (j >= 9), ct = u1 >> 3;
  const int rt = QKV ? (j >= 9 ? j - 9 : j) : (u1 & 7);
  const int colb = QKV ? 80 * hg : (ct == 6 ? 1472 : 256 * ct);
  const int g = lane >> 4, l15 = lane & 15;
  const int wm = wid & 1, wn = wid >> 1;
  auto rs = __builtin_amdgcn_make_buffer_rsrc((void*)ws, 0, WS_END, 0x00020000);
  P2SETUP();
  auto cellq = [&](int nt, int c) -> unsigned {
    const unsigned cq[8] = {0u, 40u, 176u, 216u, 0u, 336u, 376u, 0u};
    const int a = 80 * wn;
    return nt == 4 ? (c >> 1) * 176 + a + 32 + (c & 1) * 40 : nt == 7 ? 368 + a + (c & 1) * 40 + (c >> 1) * (256 * wn - 208 - 32 * (c & 1)) : cq[nt] + a + 8 * c;
  };
  unsigned wb[8];
  {
    const int c = lane & 3;
    int q = l15 >> 2, f = q | ((g & 1) << 2);
#pragma unroll
    for (int nt = 0; nt < 8; nt++) {
      unsigned cell = QKV ? cellq(nt, c) : 256 * wn + 32 * nt + 8 * c;
      wb[nt] = 16384 + (8 * g + q) * 512 + (cell ^ (f << 5));
    }
  }
  const unsigned xb0 = (64 * wm + l15) * 128 + ((g ^ (l15 >> 1)) << 4);
  f32x4 acc[8][4];
#pragma unroll
  for (int nt = 0; nt < 8; nt++)
#pragma unroll
    for (int tb = 0; tb < 4; tb++) acc[nt][tb] = f32x4{0.f, 0.f, 0.f, 0.f};
    f32x4 mP, mC, mP0; unsigned sel[2];
    {
      const float NEG = -1e30f;
#pragma unroll
      for (int r = 0; r < 4; r++) {
        int jj = 4 * g + r;
        mC[r] = (jj <= l15 && jj >= l15 - 7) ? 0.f : NEG;
        mP[r] = (jj >= l15 + 9) ? 0.f : NEG;
        mP0[r] = wm == 0 ? NEG : mP[r];
      }
#pragma unroll
      for (int e = 0; e < 2; e++) sel[e] = ((4 * g + 2 * e > l15) ? 0xffffu : 0u) | ((4 * g + 2 * e + 1 > l15) ? 0xffff0000u : 0u);
    }
    unsigned ca0 = OFF_CS + cellq(0, g) * 2, ca4 = OFF_CS + cellq(4, g) * 2;
    unsigned cv5 = OFF_CS + cellq(5, l15 >> 2) * 2 + (lane & 3) * 4, cv7 = OFF_CS + cellq(7, l15 >> 2) * 2 + (lane & 3) * 4;
    unsigned ra1 = OFF_ROW + (64 * wm + l15) * 4, ra4 = OFF_ROW + (64 * wm + 4 * g) * 4;
    const int hp = 2 * hg + wn;
    unsigned so1 = hp * 4096 + lo, so2 = g == 0 ? (16 + (hp >> 2)) * 4096 + (hp & 3) * 256 + lo : 0x40000000u;
    unsigned exw = (wn ? OFF_EX : OFF_EX0) + lane * 8 + wm * 0x10000;
    float xsc = g < 2 ? 0.22360679775f * 1.44269504089f : 1.f;
    if constexpr (QKV) asm volatile("" : "+v"(mP0), "+v"(exw), "+v"(xsc));
    if constexpr (QKV) asm volatile("" : "+v"(mP), "+v"(mC), "+v"(sel[0]), "+v"(sel[1]), "+v"(ca0), "+v"(ca4), "+v"(cv5), "+v"(cv7), "+v"(ra1), "+v"(ra4), "+v"(so1), "+v"(so2));
#pragma unroll
  for (int nt = 0; nt < 8; nt++) {
    asm volatile("" : "+v"(wb[nt]));
#pragma unroll
    for (int tb = 0; tb < 4; tb++) asm volatile("" : "+v"(acc[nt][tb]));
  }
  SB();
  {
    *(LDS u32x4*)OFF_TAG = u32x4{(unsigned)ep + 1u, (unsigned)(ep >> 32) + 0x9E3779B1u, 0u, 0u};
    *(LDS u32x4*)(OFF_TAG + 16 + (lane & 7) * 16) = u32x4{0u, 0u, 0u, 0u};
    f32x2 c2[4];
#pragma unroll
    for (int i = 0; i < 5; i++) {
#pragma unroll
      for (int d = 0; d < 4; d++) { f32x2 v = {blo(cw[i][d]), bhi(cw[i][d])}; c2[d] = i ? c2[d] + v : v; }
      SB();
    }
    float a[2] = {0.f, 0.f}, q[2] = {0.f, 0.f};
#pragma unroll
    for (int i = 0; i < 10; i++) { a[i & 1] = dot2(xs[i >> 1][i & 1], 0x3f803f80u, a[i & 1]); q[i & 1] = dot2(xs[i >> 1][i & 1], xs[i >> 1][i & 1], q[i & 1]); }
    float c[4];
#pragma unroll
    for (int k = 0; k < 4; k++) {
      auto r = __builtin_amdgcn_permlane32_swap(asu(c2[k >> 1][k & 1]), asu(c2[2 + (k >> 1)][k & 1]), false, false);
      c[k] = asf(r[0]) + asf(r[1]);
    }
    auto rs2 = __builtin_amdgcn_permlane16_swap(asu(a[0] + a[1]), asu(q[0] + q[1]), false, false);
    float z = asf(rs2[0]) + asf(rs2[1]);
#pragma unroll
    for (int k = 0; k < 2; k++) {
      auto r = __builtin_amdgcn_permlane16_swap(asu(c[k]), asu(c[k + 2]), false, false);
      c[k] = asf(r[0]) + asf(r[1]);
    }
    z += ror16<8>(z); c[0] += ror16<8>(c[0]); c[1] += ror16<8>(c[1]);
    *(LDS f32x2*)(3 * STG + wid * 256 + (lane & 7) * 32 + (lane >> 4) * 8) = f32x2{c[0], c[1]};
    z += ror16<4>(z); z += ror16<2>(z); z += ror16<1>(z);
    *(LDS float*)(3 * STG + 1024 + (4 * wid + (lane >> 4)) * 4) = z;
  }
  unsigned fo;
  const u64 tag = ((u64)(__builtin_amdgcn_readfirstlane((unsigned)(ep >> 32)) + 0x9E3779B1u) << 32) | (unsigned)(__builtin_amdgcn_readfirstlane((unsigned)ep) + 1u);
  if constexpr (!QKV) {
    mainloop<false>(acc, wb, xb0 ST_PASS);
    STAMP(15);
    const float AL = 0.46866869f, KA = 4.91222954f;
    float rs_[4], rm[4];
#pragma unroll
    for (int tb = 0; tb < 4; tb++) {
      rs_[tb] = ldsreadf(OFF_ROW + (64 * wm + 16 * tb + l15) * 4) * AL;
      rm[tb] = ldsreadf(OFF_ROW + 512 + (64 * wm + 16 * tb + l15) * 4) * rs_[tb];
    }
    const int rb = 2 * rt + wm;
    const int ks0 = 20 + (colb >> 5) + 4 * wn;
    const int skip = (colb == 1472 && wn == 0) ? 2 : 0;
    const unsigned dst = WS_A + ((p * 16 + rb) * NKS + ks0) * 4096;
    fo = WS_FH + ((p * 16 + rb) * 16 + 2 * ct + wn) * FSTR;
    const unsigned ca = OFF_CS + (128 * wn + 4 * g) * 4;
    f32x4 cc[8], bb[8];
#pragma unroll
    for (int nt = 0; nt < 8; nt++) { cc[nt] = ldsreadf4(ca + nt * 64); bb[nt] = ldsreadf4(ca + 1024 + nt * 64); }
    SB();
#pragma unroll
    for (int pr = 0; pr < 4; pr++) {
      const f32x4 c0 = cc[2 * pr], c1 = cc[2 * pr + 1], b0 = bb[2 * pr], b1v = bb[2 * pr + 1];
      const unsigned d = (pr >= skip ? dst + pr * 4096 : WS_END) + lo;
#pragma unroll
      for (int tb = 0; tb < 4; tb++) {
        f32x2 v[4], a[4];
        v[0] = fma2(bc2(rs_[tb]), lo2(acc[2 * pr][tb]), fma2(bc2(rm[tb]), lo2(c0), lo2(b0)));
        v[1] = fma2(bc2(rs_[tb]), hi2(acc[2 * pr][tb]), fma2(bc2(rm[tb]), hi2(c0), hi2(b0)));
        v[2] = fma2(bc2(rs_[tb]), lo2(acc[2 * pr + 1][tb]), fma2(bc2(rm[tb]), lo2(c1), lo2(b1v)));
        v[3] = fma2(bc2(rs_[tb]), hi2(acc[2 * pr + 1][tb]), fma2(bc2(rm[tb]), hi2(c1), hi2(b1v)));
#pragma unroll
        for (int r = 0; r < 4; r++) a[r] = -v[r] * fma2(v[r], v[r], bc2(KA));
#pragma unroll
        for (int r = 0; r < 4; r++) a[r] = fma2(f32x2{__builtin_amdgcn_exp2f(a[r][0]), __builtin_amdgcn_exp2f(a[r][1])}, bc2(AL), bc2(AL));
#pragma unroll
        for (int r = 0; r < 4; r++) v[r] *= f32x2{__builtin_amdgcn_rcpf(a[r][0]), __builtin_amdgcn_rcpf(a[r][1])};
        st16(rs, d + tb * 1024, u32x4{pk2(v[0]), pk2(v[1]), pk2(v[2]), pk2(v[3])});
      }
    }
  } else {
    mainloop<true>(acc, wb, xb0 ST_PASS);
    STAMP(15);
    const float qsc = 0.22360679775f * 1.44269504089f;
    u32x2 T[8][5];
    u32x2 XK[5];
    f32x4 cs[5]; float cv[3];
    float rs1[4], nm1[4]; f32x4 rs4[4], nm4[4];
    cs[2] = ldsreadf4(ca0 + 352); cs[3] = ldsreadf4(ca0 + 432); cs[4] = ldsreadf4(ca4); cs[0] = ldsreadf4(ca0); cs[1] = ldsreadf4(ca0 + 80);
    cv[0] = ldsreadf(cv5); cv[1] = ldsreadf(cv5 + 80); cv[2] = ldsreadf(cv7);
#pragma unroll
    for (int tq = 0; tq < 4; tq++) {
      const int tb = 3 - tq;
      nm1[tb] = ldsreadf(ra1 + 512 + 64 * tb); nm4[tb] = ldsreadf4(ra4 + 512 + 64 * tb);
      rs1[tb] = ldsreadf(ra1 + 64 * tb); rs4[tb] = ldsreadf4(ra4 + 64 * tb);
    }
    SB();
#pragma unroll
    for (int tq = 0; tq < 4; tq++) {
      const int tb = 3 - tq;
      f32x2 v[8][2];
#pragma unroll
      for (int nt = 0; nt < 8; nt++) {
        if (nt < 5) { v[nt][0] = fma2(bc2(nm1[tb]), lo2(cs[nt]), lo2(acc[nt][tb])); v[nt][1] = fma2(bc2(nm1[tb]), hi2(cs[nt]), hi2(acc[nt][tb])); }
        else { v[nt][0] = fma2(lo2(nm4[tb]), bc2(cv[nt - 5]), lo2(acc[nt][tb])); v[nt][1] = fma2(hi2(nm4[tb]), bc2(cv[nt - 5]), hi2(acc[nt][tb])); }
      }
      const float aq = rs1[tb] * qsc, ax = rs1[tb] * xsc;
      SB();
#pragma unroll
      for (int nt = 0; nt < 8; nt++) {
        if (nt < 5) { f32x2 a = bc2(nt < 2 ? aq : nt < 4 ? rs1[tb] : ax); v[nt][0] *= a; v[nt][1] *= a; }
        else { v[nt][0] *= lo2(rs4[tb]); v[nt][1] *= hi2(rs4[tb]); }
      }
      SB();
      T[4][tb + 1] = u32x2{pk2(v[4][0]), pk2(v[4][1])};
      u32x2 xa = u32x2{pk2(v[4][0]), pk2(v[4][1])}, xb = xa;
      asm volatile("" : "+v"(xa), "+v"(xb));
#pragma unroll
      for (int nt = 0; nt < 8; nt++)
        if (nt != 4) T[nt][tb + 1] = u32x2{pk2(v[nt][0]), pk2(v[nt][1])};
      SB();
#pragma unroll
      for (int e = 0; e < 2; e++) XK[tb + 1][e] = __builtin_amdgcn_permlane32_swap(xa[e], xb[e], false, false)[1];
      if (tb == 3) {
        *(LDS u32x2*)(exw) = T[2][4]; *(LDS u32x2*)(exw + 512) = T[3][4]; *(LDS u32x2*)(exw + 1024) = T[5][4];
        *(LDS u32x2*)(exw + 1536) = T[6][4]; *(LDS u32x2*)(exw + 2048) = T[7][4]; *(LDS u32x2*)((exw & ~256u) + 2560) = XK[4];
        raise(LW_HALO + wn * 4 + wm * 0x10000);
      }
      if (tb == 1) {
        wait_lds(LW_HALO + wn * 4);
        const unsigned ex = exw & 0x2ffffu;
        T[2][0] = *(LDS u32x2*)(ex); T[3][0] = *(LDS u32x2*)(ex + 512); T[5][0] = *(LDS u32x2*)(ex + 1024);
        T[6][0] = *(LDS u32x2*)(ex + 1536); T[7][0] = *(LDS u32x2*)(ex + 2048); XK[0] = *(LDS u32x2*)((ex & ~256u) + 2560);
      }
      SB();
    }
    STAMP(16);
    const u32x4 ones = {0x3f803f80u, 0x3f803f80u, 0x3f803f80u, 0x3f803f80u};
    const f32x4 z4 = {0.f, 0.f, 0.f, 0.f};
    f32x4 sp[8], sc[8];
#pragma unroll
    for (int un = 0; un < 8; un++) {
      const int tb = 3 - (un >> 1), h = un & 1;
      u32x4 Q = {T[h][tb + 1][0], T[h][tb + 1][1], g == h ? T[4][tb + 1][0] : 0u, g == h ? T[4][tb + 1][1] : 0u};
      u32x4 Kp = {T[2 + h][tb][0], T[2 + h][tb][1], XK[tb][0], XK[tb][1]};
      u32x4 Kc = {T[2 + h][tb + 1][0], T[2 + h][tb + 1][1], XK[tb + 1][0], XK[tb + 1][1]};
      sp[un] = mfma(Kp, Q, tb == 0 ? mP0 : mP); sc[un] = mfma(Kc, Q, mC);
    }
    SB();
    float mx[2][4];
#pragma unroll
    for (int un = 0; un < 8; un++)
#pragma unroll
      for (int r = 0; r < 4; r++) sp[un][r] = fmax2(sp[un][r], sc[un][r]);
    SB();
#pragma unroll
    for (int un = 0; un < 8; un++) mx[un >> 2][un & 3] = fmax2(fmax2(sp[un][0], sp[un][1]), sp[un][2]);
    SB();
#pragma unroll
    for (int un = 0; un < 8; un++) mx[un >> 2][un & 3] = fmax2(mx[un >> 2][un & 3], sp[un][3]);
    SB();
    xmax8(mx);
    SB();
    f32x2 d[8][2];
#pragma unroll
    for (int un = 0; un < 8; un++) { d[un][0] = lo2(sp[un]) - bc2(mx[un >> 2][un & 3]); d[un][1] = hi2(sp[un]) - bc2(mx[un >> 2][un & 3]); }
    SB();
#pragma unroll
    for (int un = 0; un < 8; un++)
#pragma unroll
      for (int e = 0; e < 2; e++) d[un][e] = f32x2{__builtin_amdgcn_exp2f(d[un][e][0]), __builtin_amdgcn_exp2f(d[un][e][1])};
    SB();
    u32x2 pp[8];
#pragma unroll
    for (int un = 0; un < 8; un++) pp[un] = u32x2{pk2(d[un][0]), pk2(d[un][1])};
    SB();
    f32x4 sum[8], oa[8], ob[8];
#pragma unroll
    for (int un = 0; un < 8; un++) {
      const int tb = 3 - (un >> 1), h = un & 1;
      u32x4 P = {pp[un][0] & sel[0], pp[un][1] & sel[1], pp[un][0] & ~sel[0], pp[un][1] & ~sel[1]};
      u32x4 V = {T[5 + h][tb][0], T[5 + h][tb][1], T[5 + h][tb + 1][0], T[5 + h][tb + 1][1]};
      u32x4 V2 = {T[7][tb][0], T[7][tb][1], T[7][tb + 1][0], T[7][tb + 1][1]};
      sum[un] = mfma(ones, P, z4); oa[un] = mfma(V, P, z4); ob[un] = mfma(V2, P, z4);
    }
    SB();
    float ri[8];
#pragma unroll
    for (int un = 0; un < 8; un++) ri[un] = __builtin_amdgcn_rcpf(sum[un][0]);
    SB();
#pragma unroll
    for (int k = 0; k < 4; k++) {
      const int tb = 3 - k;
      f32x2 w[8];
#pragma unroll
      for (int h = 0; h < 2; h++) {
        f32x2 r = bc2(ri[2 * k + h]);
        w[4 * h] = lo2(ob[2 * k + h]) * r; w[4 * h + 1] = hi2(ob[2 * k + h]) * r; w[4 * h + 2] = lo2(oa[2 * k + h]) * r; w[4 * h + 3] = hi2(oa[2 * k + h]) * r;
      }
      const int B = 7 * rt + 4 * wm + tb;
      unsigned dst = WS_A + ((p * 16 + (B >> 2)) * NKS) * 4096 + (B & 3) * 1024;
      if (tb == 0) dst = (wm == 0 && rt != 0) ? WS_END : dst;
      unsigned d1 = dst + so1, d2 = dst + so2;
      asm volatile("" : "+v"(d1), "+v"(d2));
      SB();
      u32x2 ya = u32x2{pk2(w[4]), pk2(w[5])}, yb = u32x2{0u, 0u};
      u32x4 o1 = {pk2(w[2]), pk2(w[3]), pk2(w[6]), pk2(w[7])};
      u32x2 o2 = {pk2(w[0]), pk2(w[1])};
      SB();
      st16(rs, d1, o1);
      unsigned r0 = __builtin_amdgcn_permlane16_swap(ya[0], yb[0], false, false)[1], r1 = __builtin_amdgcn_permlane16_swap(ya[1], yb[1], false, false)[1];
      st16(rs, d2, u32x4{o2[0], o2[1], r0, r1});
      SB();
    }
    fo = WS_FQ + ((p * 18 + 2 * rt + wm) * 16 + hp) * FSTR;
  }
  STAMP(17);
  SB();
  phase2_y<QKV ? QPRE : WPRE, QKV ? QTRIG : WTRIG>(rs, t2, wid, lane, fo, tag ST_PASS);
  ST_END(ws, 0);
  __builtin_amdgcn_endpgm();
}

#define DD(k) ((k) == 0 ? D0 : (k) >= 9 ? 0 : D1 + ((D8 - D1) * ((k) - 1)) / 7)
#define POS(k) (12 * ((k) + 1) + DD(k))
template <bool QKV>
DEV void load_role(const char* xg, const char* wg, unsigned xv0, unsigned xv, unsigned wv0, unsigned wv1, const u16* b1, const u16* wo, const u16* w2, const u16* x, const u16* b2, u16* out, char* ws, int b, int wid, int lane ST_ARG) {
  constexpr int D0 = QKV ? QD0 : WD0, D1 = QKV ? QD1 : WD1, D8 = QKV ? QD8 : 12;
  constexpr int ldw = QKV ? NQKV * 2 : F_ * 2;
  constexpr int PC = QKV ? QPACE : WPACE, PC5 = PC > 4 ? PC - 4 : 0;
  const int xcd = b & 7, j = b >> 3, p = xcd & 1, qd = xcd >> 1;
  const int u1 = 14 * qd + (j - 18);
  const int hg = 2 * qd + (j >= 9), ct = u1 >> 3;
  const int rt = QKV ? (j >= 9 ? j - 9 : j) : (u1 & 7);
  const int i0 = (QKV ? 112 : 128) * rt;
  const int colb = QKV ? 80 * hg : (ct == 6 ? 1472 : 256 * ct);
  const int g = lane >> 4, l15 = lane & 15, lw = wid & 3;
  const int cq = (b * 1150) >> 16, cgp = b - cq * 57;
  STAMP(5);
  auto rs = __builtin_amdgcn_make_buffer_rsrc((void*)ws, 0, WS_END, 0x00020000);
  P2SETUP();
  const u32x4 xsr = mksrd(xg), wsr = mksrd(wg);
#define run(a, e) do { Run<(a), (e), ldw>::go(lw * 1024, xv0, xv, wv0, wv1, xsr, wsr); SB(); } while (0)
  unsigned goff = WS_G + (p * 1024 + i0 + lw * 32 + (lane & 31)) * 16;
  unsigned foff = WS_FW + lane * 16;
  unsigned boff = ((QKV ? 0 : colb) + lw * 64 + lane) * 2;
  unsigned coff = WS_CG + (QKV ? colb + (qkvoff(lw * 8 + (lane >> 3)) >> 1) + (lane & 7) : NQKV + colb + lw * 64 + lane) * 8;
  unsigned pg = (lane < 8 && lw == 0) ? WS_G + (p * 1024 + 8 * (b >> 1) + lane) * 16 : WS_END;
  unsigned pc_ = (b < 228 && lw == 1) ? WS_CG + (cq * 3648 + cgp * 64 + lane) * 8 : WS_END;
  unsigned la0 = OFF_ROW + (lane >> 5) * 512 + (lw * 32 + (lane & 31)) * 4, la1 = OFF_CS + (lw * 64 + lane) * 4;
  asm volatile("" : "+v"(la0), "+v"(la1), "+v"(goff), "+v"(foff), "+v"(boff), "+v"(coff), "+v"(pg), "+v"(pc_));
  auto xr = __builtin_amdgcn_make_buffer_rsrc((void*)x, 0, 0x7fffffff, 0x00020000);
  auto br = __builtin_amdgcn_make_buffer_rsrc((void*)b2, 0, 0x7fffffff, 0x00020000);
  unsigned go[5], bo[5], poff;
  auto late_setup = [&]() {
#pragma unroll
    for (int r = 0; r < 5; r++) {
      int uu = r * 256 + lw * 64 + lane;
      int tk = (uu * 3277) >> 16, c = uu - tk * 20;
      c ^= (tk >> 1) & 3;
      bo[r] = 160 * t2.cb + 8 * c;
      go[r] = (2 * (64 * t2.rb + tk) + t2.p) * 1280 + bo[r];
      PIN(go[r]); PIN(bo[r]);
    }
    int B = 4 * t2.rb + ((lane & 16) ? 3 : 0);
    int r = ((B + 6) * 147 >> 10) - 1; r = r < 0 ? 0 : r;
    poff = WS_FQ + ((t2.p * 18 + 2 * r + ((B - 7 * r) >= 4)) * 16 + l15) * FSTR;
    unsigned hoff = WS_FH + ((t2.p * 16 + t2.rb) * 16 + (l15 < 14 ? l15 : 0)) * FSTR;
    asm volatile("" : "+v"(poff), "+v"(hoff));
    poff = lane < 32 ? poff : hoff;
    PIN(poff);
    SB();
  };
  const int u = b * 3 + lw, rks = (u * 6554) >> 16, rcg = u - rks * 10;
  const bool rtl = lw < 3 && u < 740, mid = rks >= 16 && rks < 20;
  const u32x4 rsr = mksrd((const char*)(rks < 20 ? wo : w2) + rcg * 128 + ((mid ? 160 : rks < 16 ? 40 : 32) * rks + (mid ? -2544 : rks < 16 ? 0 : -640)) * 1280);
  const int rdt = (rks < 20 ? 20 : 16) * 1280, rdg = (mid ? 40 : 4) * 1280;
  unsigned rv = ((lane >> 5) * rdg + ((lane >> 3) & 3) * 1280 + (((lane & 7) ^ (g << 1)) << 4)) | (rtl ? 0u : 0x80000000u);
  u32x2 rlo[4], rhi[4]; unsigned rra[4], rso[4];
#pragma unroll
  for (int nt = 0; nt < 4; nt++) {
    const int row = 4 * (g & 1) + (l15 >> 2);
    rra[nt] = 2 * STG + lw * 1024 + (g >> 1) * 4096 + row * 128 + ((nt ^ (row >> 1)) << 5) + (lane & 3) * 8;
    int NT = 4 * rcg + nt, cb = (NT * 205) >> 10;
    rso[nt] = lo + (rtl ? WS_W + ((cb * (NKS - 1) + rks) * 5 + NT) * 1024 : WS_END);
    PIN(rra[nt]); PIN(rso[nt]);
  }
  PIN(rv);
  auto rload = [&]() {
#pragma unroll
    for (int i = 0; i < 4; i++) dmas(2 * STG + lw * 1024 + i * 4096, rv, rsr, (i >> 1) * rdt + (i & 1) * 2 * rdg);
    SB();
  };
  auto put = [&](int nt) { st16(rs, rso[nt], cat(rlo[nt], rhi[nt])); SB(); };
  run(NEARLY, 12);
  rload();
  run(12, POS(0));
  late_setup();
  STAMP(2);
  WAITV(DD(0) + 4);
  STAMP(3);
  BAR();
  const u32x2 tg = *(LDS u32x2*)OFF_TAG;
  const u32x2 st = *(LDS u32x2*)(3 * STG + 1024 + (lane & 7) * 8);
  const float c0 = ldsreadf(3 * STG + lane * 4), c1 = ldsreadf(3 * STG + 256 + lane * 4), c2 = ldsreadf(3 * STG + 512 + lane * 4), c3 = ldsreadf(3 * STG + 768 + lane * 4);
  SB();
  run(POS(0), 24);
  WAITV(RPOS == 0 ? 12 : 12 - DD(0));
#pragma unroll
  for (int nt = 0; nt < 4; nt++) { rlo[nt] = trread(rra[nt]); rhi[nt] = trread(rra[nt] + 8192); }
  asm volatile("s_waitcnt lgkmcnt(0)" ::: "memory");
  SB();
  run(24, POS(1));
  const unsigned t0 = __builtin_amdgcn_readfirstlane(tg[0]), t1 = __builtin_amdgcn_readfirstlane(tg[1]);
  const u64 tag = ((u64)t1 << 32) | t0;
  WAITV(DD(1));
  BAR();
  run(POS(1), POS(2) - 4);
  if (lw == 0) {
    float mean = asf(st[0]) * (1.f / 640.f);
    float var = fmaxf(fmaf(asf(st[1]), 1.f / 640.f, -mean * mean), 0.f);
    st16(rs, pg, u32x4{asu(__builtin_amdgcn_rsqf(var + 1e-5f)), t0, asu(-mean), t1});
  }
  else __builtin_amdgcn_raw_buffer_store_b64(u32x2{asu(add1(add1(c0, c1), add1(c2, c3))), t0}, rs, pc_, 0, 16);
  SB();
  run(POS(2) - 4, POS(2) - 2); put(0); run(POS(2) - 2, POS(2)); put(1);
  WAITV(DD(2) + 3);
  BAR();
  run(POS(2), POS(3) - 2); put(2); run(POS(3) - 2, POS(3)); put(3);
  WAITV(DD(3) + 2);
  BAR();
  run(POS(3), POS(4));
  WAITV(DD(4) < POS(4) - POS(3) ? DD(4) : POS(4) - POS(3));
  BAR();
  if (lw == 0) st_flag1(rs, lane, WS_FW + b * 8, tag);
  SB();
  run(POS(4), POS(5));
  WAITV(DD(5));
  BAR();
  u32x4 gr, fw, fx; unsigned bias; u32x2 cg[4];
#define SIDE_LOADS(tail)                                                                                                                     \
  asm volatile("global_load_dwordx4 %0, %7, %10 sc1\n\tglobal_load_dwordx4 %1, %8, %10 sc1\n\tglobal_load_dwordx4 %6, %8, %10 offset:1024 sc1\n\t" \
               "global_load_dwordx2 %2, %9, %10 sc1\n\tglobal_load_dwordx2 %3, %9, %11 sc1\n\tglobal_load_dwordx2 %4, %9, %12 sc1\n\t"       \
               "global_load_dwordx2 %5, %9, %13 sc1" tail                                                                                    \
               : "=&v"(gr), "=&v"(fw), "=&v"(cg[0]), "=&v"(cg[1]), "=&v"(cg[2]), "=&v"(cg[3]), "=&v"(fx)                                     \
               : "v"(goff), "v"(foff), "v"(coff), "s"(ws), "s"(ws + 3648 * 8), "s"(ws + 2 * 3648 * 8), "s"(ws + 3 * 3648 * 8) : "memory")
  auto side = [&]() {
    SIDE_LOADS("");
    asm volatile("global_load_ushort %0, %1, %2" : "=&v"(bias) : "v"(boff), "s"(b1) : "memory");
  };
  auto check = [&]() {
    asm volatile("" : "+v"(gr), "+v"(fw), "+v"(fx), "+v"(bias), "+v"(cg[0]), "+v"(cg[1]), "+v"(cg[2]), "+v"(cg[3])::"memory");
    STAMP(8);
#pragma unroll 1
    for (int it = 0; it < SPIN_CAP; it++) {
      unsigned bd = (gr[1] ^ t0) | (gr[3] ^ t1) | (fw[0] ^ t0) | (fw[1] ^ t1) | (fw[2] ^ t0) | (fw[3] ^ t1) | (cg[0][1] ^ t0) | (cg[1][1] ^ t0) |
                    (cg[2][1] ^ t0) | (cg[3][1] ^ t0) | (fx[0] ^ t0) | (fx[1] ^ t1) | (fx[2] ^ t0) | (fx[3] ^ t1);
      PIN(bd);
      if (__builtin_amdgcn_ballot_w64(bd != 0) == 0) break;
      __builtin_amdgcn_s_sleep(1);
      SIDE_LOADS("\n\ts_waitcnt vmcnt(0)");
    }
    asm volatile("" ::: "memory");
    if (b == 255 && lw == 3) st_flag1(rs, lane, WS_C, tag);
    SB();
  };
  auto tables = [&]() {
    *(LDS unsigned*)(la0) = (lane >> 5) ? gr[2] : gr[0];
    *(LDS float*)(la1 + 1024) = asf(bias << 16) * 0.46866869f;
    *(LDS float*)(la1) = add1(add1(asf(cg[0][0]), asf(cg[1][0])), add1(asf(cg[2][0]), asf(cg[3][0])));
    SB();
  };
  u32x4 wr[5][5];
  auto pre = [&](int s) { ldW(wr[s], rs, lo, wcol + sstep(t2, lw, s) * 5120); SB(); };
  constexpr int NPRE = QKV ? QPRE : WPRE;
  auto fill = [&](int k, bool paced) {
    const char* src = ws + wcol + ystep(t2, lw, NPRE + k, NPRE + 7) * 5120;
#pragma unroll
    for (int i = 0; i < 5; i++) { if (paced) WAITV(PC); dma16c((lw + (k >> 2) * 4) * 20480 + (k & 3) * 5120 + i * 1024, lo, src + i * 1024); }
  };
  u32x2 rx_[5], rb_[5];
  auto resid = [&]() {
#pragma unroll
    for (int r = 0; r < 5; r++) rb_[r] = __builtin_amdgcn_raw_buffer_load_b64(br, bo[r], 0, 0);
    SB();
  };
  run(POS(5), POS(6));
  WAITV(DD(6));
  BAR();
  const int n = lw >= 2 ? 7 : 8;
  if constexpr (QKV) {
    run(POS(6), POS(7));
    WAITV(DD(7));
    BAR();
    run(POS(7), POS(8));
    WAITV(DD(8));
    BAR();
    STAMP(6);
    run(POS(8), 120);
    side();
    resid();
    VMCNT(13);
    STAMP(7);
    BAR();
    VMCNT(5);
    check();
    pre(0);
    tables();
    STAMP(9);
    BAR();
    STAMP(16);
    WAITV(PC5); pre(1); WAITV(PC5); pre(2); WAITV(PC5); pre(3); WAITV(PC5); pre(4);
#pragma unroll 1
    for (int k = 0; k < n; k++) fill(k, true);
  } else {
    run(POS(6), POS(7));
    side();
    WAITV(DD(7) + 8);
    BAR();
    run(POS(7), POS(8));
    resid();
    WAITV(17);
    BAR();
    STAMP(6);
    check();
    pre(0); pre(1);
    tables();
    if (WI8) pre(2);
    WAITV(WI8 ? 20 : 15);
    STAMP(7);
    BAR();
    if (!WI8) pre(2);
    pre(3); pre(4);
    if (WI8) fill(4, false);
    STAMP(9);
    BAR();
    STAMP(16);
#pragma unroll 1
    for (int k = 0; k < n; k++) if (k != 4 || !WI8) fill(k, true);
  }
  STAMP(17);
  VMCNT(0);
  raise(LW_FILL + lw * 4);
  STAMP(18);
  wait_lds(LW_EARLY);
#pragma unroll
  for (int r = 0; r < 5; r++) rx_[r] = __builtin_amdgcn_raw_buffer_load_b64(xr, go[r], 0, 0);
  SB();
  if (lw == 0) poll2(ws, poff, tag);
  phase2_s(wr, rx_, rb_, go, rs, out, t2, lw, lane ST_PASS);
  ST_END(ws, 0);
  __builtin_amdgcn_endpgm();
}

typedef u64 u64x8 __attribute__((ext_vector_type(8)));
typedef u64 u64x4 __attribute__((ext_vector_type(4)));
#define GPTR(T, v) ((T*)(__attribute__((address_space(1))) T*)(v))
__global__ __launch_bounds__(512, 2) void fused(const u16* a0, const u16* a1, const u16* a2, char* a3, const u16* a4, const u16* a5, const u16* a6,
                                                const u16* a7, u16* a8) {
  extern __shared__ __attribute__((aligned(16))) char smem[];
  const int tid = threadIdx.x;
  const int wid = __builtin_amdgcn_readfirstlane(tid >> 6);
  int b = blockIdx.x, lane = tid & 63;
  u64 pc; asm volatile("s_getpc_b64 %0" : "=s"(pc));
  ST_INIT();
  u64x8 kl; u64 kb; unsigned xv, xv0, wv[2]; const char *xg, *wg;
  if (wid >= 4) {
    u64x8 ka;
    asm volatile("s_load_dwordx16 %0, %3, 0x0\n\ts_load_dwordx2 %1, %3, 0x40" : "=&s"(ka), "=&s"(kb), "+s"(b) : "s"(__builtin_amdgcn_kernarg_segment_ptr()));
    const int xcd = b & 7, j = b >> 3, p = xcd & 1, qd = xcd >> 1, lw = wid & 3;
    const bool isq = j < 18;
    const int u1 = 14 * qd + (j - 18);
    const int h9 = (j + 7) >> 4, ct = u1 >> 3;
    int rq = j - 9 * h9, cq_ = 80 * (2 * qd + h9), cw_ = min(256 * ct, 1472);
    asm volatile("" : "+s"(rq), "+s"(cq_), "+s"(cw_));
    const int rt = isq ? rq : (u1 & 7);
    const int i0 = (isq ? 112 : 128) * rt;
    const int colb = isq ? cq_ : cw_;
    xv = (lane >> 3) * 2560 + (((lane & 7) ^ ((4 * lw + (lane >> 4)) & 7)) << 4);
    const int ldw = isq ? NQKV * 2 : F_ * 2;
    const unsigned zm = isq ? 0x80000400u : ct == 6 ? 0xffu : 0u;
    const int g1 = isq ? 1280 - 176 : 0, g2 = isq ? 1280 - 160 : 0;
#pragma unroll
    for (int e = 0; e < 2; e++) {
      int hi = lane >> 5, c = (lane & 31) ^ ((((2 * lw + hi) & 3) | (e << 2)) << 1);
      wv[e] = (hi * ldw + c * 16 + (c > 10 ? g1 : 0) + (c > 20 ? g2 : 0)) | ((zm >> c) << 31);
    }
    unsigned xso = (2 * (i0 + 8 * lw) + p) * 1280, wso = 2 * lw * ldw + colb * 2;
    xv0 = xv | ((isq && rt != 0 && lw == 0) ? 0x80000000u : 0u);
    asm volatile("" : "+v"(wv[0]), "+v"(wv[1]), "+v"(xv), "+v"(xv0));
    asm volatile("s_waitcnt lgkmcnt(0)" : "+s"(ka), "+s"(kb), "+s"(xso), "+s"(wso));
    STAMP(4);
    xg = GPTR(const char, ka[0]) + xso;
    wg = GPTR(const char, isq ? ka[1] : ka[2]) + wso;
    const u32x4 xsr = mksrd(xg), wsr = mksrd(wg);
#pragma unroll
    for (int i = 0; i < NEARLY / 3; i++) {
      const unsigned so = (lw + 4 * i) * 1024;
      dmas(so, i == 0 ? xv0 : xv, xsr, i * (64 * 1280));
      dmas(so + 16384 + i * 4096, wv[0], wsr, i * 16 * ldw);
      dmas(so + 16384 + i * 4096 + 4096, wv[1], wsr, (i * 16 + 8) * ldw);
    }
    kl = ka;
  }
  int role = wid;
  asm volatile("" : "+s"(role));
  if (__builtin_expect(role >= 4, 0)) {
    const bool isq = (b >> 3) < 18;
    const u16 *x = GPTR(const u16, kl[0]), *b1 = GPTR(const u16, kl[4]), *wo = GPTR(const u16, kl[5]), *w2 = GPTR(const u16, kl[6]), *b2 = GPTR(const u16, kl[7]);
    char* ws = GPTR(char, kl[3]); u16* out = GPTR(u16, kb);
    if (isq) load_role<true>(xg, wg, xv0, xv, wv[0], wv[1], b1, wo, w2, x, b2, out, ws, b, wid, lane ST_PASS);
    else load_role<false>(xg, wg, xv0, xv, wv[0], wv[1], b1, wo, w2, x, b2, out, ws, b, wid, lane ST_PASS);
  }
  u64x4 ka;
  asm volatile("s_load_dwordx8 %0, %2, 0x0" : "=&s"(ka), "+s"(b) : "s"(__builtin_amdgcn_kernarg_segment_ptr()));
  if (wid == 0) {
    const unsigned ko = min((unsigned)((b >> 3) + 32 * (lane & 15)) * 128u, (unsigned)CODE_SPAN);
    asm volatile("s_mov_b32 m0, %0\n\ts_nop 0\n\tglobal_load_lds_dword %1, %2" ::"s"(3 * STG + 2048), "v"(ko), "s"(pc) : "memory");
  }
  const int cq = (b * 1150) >> 16, cgp = b - cq * 57;
  int cld = cgp < 30 ? NQKV * 2 : F_ * 2;
  unsigned sxo = (2 * (8 * (b >> 1) + 2 * wid + (lane >> 5)) + (b & 1)) * 1280 + (lane & 31) * 8;
  unsigned sco = (cgp < 30 ? cgp : cgp - 30) * 128 + ((cq & 3) * 160 + 40 * wid + (lane >> 3)) * cld + (lane & 7) * 16;
  asm volatile("" : "+v"(sxo), "+v"(sco));
  asm volatile("s_waitcnt lgkmcnt(0)" : "+s"(ka), "+s"(cld));
  STAMP(4);
  char* ws = GPTR(char, ka[3]);
  auto xr = __builtin_amdgcn_make_buffer_rsrc(GPTR(char, ka[0]), 0, 0x7fffffff, 0x00020000);
  auto cr = __builtin_amdgcn_make_buffer_rsrc(GPTR(char, cgp < 30 ? ka[1] : ka[2]), 0, 0x7fffffff, 0x00020000);
  u32x2 xs[5]; u32x4 cw[5];
  const u64 ep = ld_flag(ws, WS_C);
#pragma unroll
  for (int i = 0; i < 5; i++) cw[i] = __builtin_amdgcn_raw_buffer_load_b128(cr, sco, i * 8 * cld, 0);
#pragma unroll
  for (int i = 0; i < 5; i++) xs[i] = __builtin_amdgcn_raw_buffer_load_b64(xr, sxo + i * 256, 0, 0);
  SB();
  STAMP(5);
  if ((b >> 3) < 18) mfma_role<true>(xs, cw, ep, ws, b, wid, lane ST_PASS);
  else mfma_role<false>(xs, cw, ep, ws, b, wid, lane ST_PASS);
}

extern "C" void kernel_launch(void* const* d_in, const int* in_sizes, int n_in, void* d_out, int out_size, void* d_ws, size_t ws_size,
                              hipStream_t stream) {
  const u16* x = (const u16*)d_in[0]; const u16* wqkv = (const u16*)d_in[1]; const u16* wo = (const u16*)d_in[2];
  const u16* w1 = (const u16*)d_in[4]; const u16* b1 = (const u16*)d_in[5]; const u16* w2 = (const u16*)d_in[6]; const u16* b2 = (const u16*)d_in[7];
  (void)hipFuncSetAttribute((const void*)fused, hipFuncAttributeMaxDynamicSharedMemorySize, LDS_BYTES);
  fused<<<256, 512, LDS_BYTES, stream>>>(x, wqkv, w1, (char*)d_ws, b1, wo, w2, b2, (u16*)d_out);
}
```
